```python
import torch

def _softmax(x, axis=-1):
    return torch.softmax(x, dim=axis)
def _gelu(x):
    return torch.nn.functional.gelu(x, approximate="tanh")
def _silu(x):
    return torch.nn.functional.silu(x)
def _sigmoid(x):
    return torch.sigmoid(x)
def _layernorm(x, axis=-1, eps=1e-5):
    m = x.mean(dim=axis, keepdim=True)
    v = x.var(dim=axis, keepdim=True, unbiased=False)
    return (x - m) / torch.sqrt(v + eps)
def _scatter_add(tgt, idx, src, axis=0):
    return tgt.clone().index_add_(axis, idx, src)
def _segment_sum(src, seg, n):
    out = torch.zeros((n,) + src.shape[1:], dtype=src.dtype)
    return out.index_add_(0, seg, src)
def _pad(x, pw):
    flat = tuple(v for pair in reversed(pw) for v in pair)
    return torch.nn.functional.pad(x, flat)

kernel_name = 'concat_heads__compressed_kv_cla__ssm_conv_s5__prompt'

def setup_inputs():

    _w = (lambda a: (lambda t: t.to(torch.bfloat16) if t.is_floating_point() else t)(torch.as_tensor(a)))
    def _A_setup():
        B, H, Sq, Sk, D, M = 1, 32, 2048, 2048, 128, 512
        Dc = 4 * D
        return {
            "x": _w((lambda *s: torch.randn(*s, dtype=torch.bfloat16) * 3.2)(B, Sq, M)),
            "c_kv": _w((lambda *s: torch.randn(*s, dtype=torch.bfloat16) * 0.1)(B, Sk, Dc)),
            "w_q": _w((lambda *s: torch.randn(*s, dtype=torch.bfloat16) * 6.4)(M, H, D) * (M ** -0.5)),
            "w_uk": _w((lambda *s: torch.randn(*s, dtype=torch.bfloat16) * 6.4)(Dc, H, D) * (Dc ** -0.5)),
            "w_uv": _w((lambda *s: torch.randn(*s, dtype=torch.bfloat16) * 0.1)(Dc, H, D) * (Dc ** -0.5)),
            "w_o": _w((lambda *s: torch.randn(*s, dtype=torch.bfloat16) * 0.1)(H, D, M) * ((H * D) ** -0.5)),
        }
    _A_in = _A_setup()

    def _B_setup():
        B, S, D = 1, 2048, 512
        P = D
        Lambda_re = -0.5 * (lambda _x: torch.exp(torch.as_tensor(_x)))((lambda *s: torch.randn(*s, dtype=torch.bfloat16) * 0.2)(P))
        Lambda_im = (lambda *s: torch.randn(*s, dtype=torch.bfloat16) * 0.4)(P) * 1.0
        B_re = (lambda *s: torch.randn(*s, dtype=torch.bfloat16) * 0.2)(P, D) * (D ** -0.5)
        B_im = (lambda *s: torch.randn(*s, dtype=torch.bfloat16) * 0.2)(P, D) * (D ** -0.5)
        C_re = (lambda *s: torch.randn(*s, dtype=torch.bfloat16) * 0.2)(D, P) * (P ** -0.5)
        C_im = (lambda *s: torch.randn(*s, dtype=torch.bfloat16) * 0.2)(D, P) * (P ** -0.5)
        log_dt = (lambda *s: torch.randn(*s, dtype=torch.bfloat16) * 0.2)(P) * 0.3 - 2.0
        Dskip = (lambda *s: torch.randn(*s, dtype=torch.bfloat16) * 0.05)(D)
        return {
            "x": _w((lambda *s: torch.randn(*s, dtype=torch.bfloat16) * 0.05)(B, S, D)),
            "Lambda_re": _w(Lambda_re),
            "Lambda_im": _w(Lambda_im),
            "B_re": _w(B_re),
            "B_im": _w(B_im),
            "C_re": _w(C_re),
            "C_im": _w(C_im),
            "log_dt": _w(log_dt),
            "Dskip": _w(Dskip),
        }
    _B_in = _B_setup()

    return {
        "A_x": _A_in["x"],
        "A_c_kv": _A_in["c_kv"],
        "A_w_q": _A_in["w_q"],
        "A_w_uk": _A_in["w_uk"],
        "A_w_uv": _A_in["w_uv"],
        "A_w_o": _A_in["w_o"],
        "B_x": _B_in["x"],
        "B_Lambda_re": _B_in["Lambda_re"],
        "B_Lambda_im": _B_in["Lambda_im"],
        "B_B_re": _B_in["B_re"],
        "B_B_im": _B_in["B_im"],
        "B_C_re": _B_in["C_re"],
        "B_C_im": _B_in["C_im"],
        "B_log_dt": _B_in["log_dt"],
        "B_Dskip": _B_in["Dskip"],
    }

def reference(A_x, A_c_kv, A_w_q, A_w_uk, A_w_uv, A_w_o, B_x, B_Lambda_re, B_Lambda_im, B_B_re, B_B_im, B_C_re, B_C_im, B_log_dt, B_Dskip):
    def _A_ref(x, c_kv, w_q, w_uk, w_uv, w_o):
        B, Sq, M = x.shape[0], x.shape[1], x.shape[2]
        Sk = c_kv.shape[1]
        H, D = w_q.shape[1], w_q.shape[2]
        q = torch.einsum("bqm,mhd->bhqd", x, w_q)
        k = torch.einsum("bkc,chd->bhkd", c_kv, w_uk)
        v = torch.einsum("bkc,chd->bhkd", c_kv, w_uv)
        scores = torch.einsum("bhqd,bhkd->bhqk", q, k) * (D ** -0.5)
        off = Sk - Sq
        qi = torch.arange(Sq)[:, None]
        kj = torch.arange(Sk)[None, :]
        cmask = kj <= (qi + off)
        scores = torch.where(cmask[None, None], scores, -1e9)
        p = _softmax(scores, axis=-1)
        ctx = torch.einsum("bhqk,bhkd->bhqd", p, v)
        return torch.einsum("bhqd,hdm->bqm", ctx, w_o)

    def _B_ref(x, Lambda_re, Lambda_im, B_re, B_im, C_re, C_im, log_dt, Dskip):
        Bsz, L, H = x.shape
        Lam = Lambda_re + 1j * Lambda_im
        Bmat = B_re + 1j * B_im
        Cmat = C_re + 1j * C_im
        dt = torch.exp(log_dt)
        Lam_bar = torch.exp(dt * Lam)
        B_bar = ((Lam_bar - 1.0) / Lam)[:, None] * Bmat
        Bu = torch.einsum("bld,pd->blp", x + 0j, B_bar)
        h = torch.zeros_like(Bu[:, 0, :])
        hs = []
        for t in range(L):
            h = Lam_bar[None, :] * h + Bu[:, t, :]
            hs.append(h)
        hseq = torch.stack(hs, 1)
        y = 2.0 * torch.einsum("blp,dp->bld", hseq, Cmat).real + x * Dskip[None, None, :]
        return y

    a = _A_ref(A_x, A_c_kv, A_w_q, A_w_uk, A_w_uv, A_w_o)
    b = _B_ref(B_x, B_Lambda_re, B_Lambda_im, B_B_re, B_B_im, B_C_re, B_C_im, B_log_dt, B_Dskip)
    return torch.concatenate([a, b], axis=-1)
```

```cpp
#include <hip/hip_runtime.h>
#include <hip/hip_bf16.h>
#include <stdint.h>

typedef unsigned short u16;
__device__ __forceinline__ float bf2f(u16 v) { return __uint_as_float(((unsigned)v) << 16); }
__device__ __forceinline__ u16 f2bf(float f) { unsigned u = __float_as_uint(f); u += 0x7FFFu + ((u >> 16) & 1u); return (u16)(u >> 16); }

__global__ void gemm_kn(const u16* A, const u16* B, u16* C, int M, int N, int K) {
    int n = blockIdx.x * blockDim.x + threadIdx.x, m = blockIdx.y;
    float acc = 0.f;
    for (int k = 0; k < K; ++k) acc += bf2f(A[(size_t)m * K + k]) * bf2f(B[(size_t)k * N + n]);
    C[(size_t)m * N + n] = f2bf(acc);
}
__global__ void gemm_out(const u16* A, const u16* B, float* C, int M, int N, int K) {
    int n = blockIdx.x * blockDim.x + threadIdx.x, m = blockIdx.y;
    float acc = 0.f;
    for (int k = 0; k < K; ++k) acc += bf2f(A[(size_t)m * K + k]) * bf2f(B[(size_t)k * N + n]);
    C[(size_t)m * 1024 + n] = acc;
}
__global__ void attn_naive(const u16* Q, const u16* Kk, const u16* V, u16* ctx) {
    __shared__ float s[2048];
    __shared__ float qs[128];
    __shared__ float red[128];
    int q = blockIdx.x, h = blockIdx.y, t = threadIdx.x;
    qs[t] = bf2f(Q[(size_t)q * 4096 + h * 128 + t]);
    __syncthreads();
    float mx = -1e30f;
    for (int j = t; j <= q; j += 128) {
        float acc = 0.f;
        for (int d = 0; d < 128; ++d) acc += qs[d] * bf2f(Kk[(size_t)j * 4096 + h * 128 + d]);
        acc *= 0.08838834764831845f;
        s[j] = acc; mx = fmaxf(mx, acc);
    }
    red[t] = mx; __syncthreads();
    for (int o = 64; o > 0; o >>= 1) { if (t < o) red[t] = fmaxf(red[t], red[t + o]); __syncthreads(); }
    mx = red[0]; __syncthreads();
    float sum = 0.f;
    for (int j = t; j <= q; j += 128) { float p = __expf(s[j] - mx); s[j] = p; sum += p; }
    red[t] = sum; __syncthreads();
    for (int o = 64; o > 0; o >>= 1) { if (t < o) red[t] += red[t + o]; __syncthreads(); }
    sum = red[0];
    float acc = 0.f;
    for (int j = 0; j <= q; ++j) acc += s[j] * bf2f(V[(size_t)j * 4096 + h * 128 + t]);
    ctx[(size_t)q * 4096 + h * 128 + t] = f2bf(acc / sum);
}
__global__ void bu_naive(const u16* x, const u16* Bre, const u16* Bim, const u16* Lre, const u16* Lim, const u16* logdt, float2* Bu) {
    int p = blockIdx.x * blockDim.x + threadIdx.x, l = blockIdx.y;
    float ar = 0.f, ai = 0.f;
    for (int d = 0; d < 512; ++d) { float xv = bf2f(x[(size_t)l * 512 + d]); ar += xv * bf2f(Bre[(size_t)p * 512 + d]); ai += xv * bf2f(Bim[(size_t)p * 512 + d]); }
    float lr = bf2f(Lre[p]), li = bf2f(Lim[p]), dt = expf(bf2f(logdt[p]));
    float e = expf(dt * lr), br = e * cosf(dt * li), bi = e * sinf(dt * li);
    float nr = br - 1.f, ni = bi, den = lr * lr + li * li;
    float cr = (nr * lr + ni * li) / den, ci = (ni * lr - nr * li) / den;
    Bu[(size_t)l * 512 + p] = make_float2(cr * ar - ci * ai, cr * ai + ci * ar);
}
__global__ void scan_naive(float2* Bu, const u16* Lre, const u16* Lim, const u16* logdt) {
    int p = blockIdx.x * blockDim.x + threadIdx.x;
    float lr = bf2f(Lre[p]), li = bf2f(Lim[p]), dt = expf(bf2f(logdt[p]));
    float e = expf(dt * lr), br = e * cosf(dt * li), bi = e * sinf(dt * li);
    float hr = 0.f, hi = 0.f;
    for (int l = 0; l < 2048; ++l) {
        float2 u = Bu[(size_t)l * 512 + p];
        float nr = br * hr - bi * hi + u.x, ni = br * hi + bi * hr + u.y;
        hr = nr; hi = ni;
        Bu[(size_t)l * 512 + p] = make_float2(hr, hi);
    }
}
__global__ void y_naive(const float2* h, const u16* Cre, const u16* Cim, const u16* x, const u16* Dsk, float* out) {
    int d = blockIdx.x * blockDim.x + threadIdx.x, l = blockIdx.y;
    float acc = 0.f;
    for (int p = 0; p < 512; ++p) { float2 hv = h[(size_t)l * 512 + p]; acc += hv.x * bf2f(Cre[(size_t)d * 512 + p]) - hv.y * bf2f(Cim[(size_t)d * 512 + p]); }
    out[(size_t)l * 1024 + 512 + d] = 2.f * acc + bf2f(x[(size_t)l * 512 + d]) * bf2f(Dsk[d]);
}

extern "C" void kernel_launch(void* const* d_in, const int* in_sizes, int n_in,
                              void* d_out, int out_size, void* d_ws, size_t ws_size,
                              hipStream_t stream) {
    const u16* Ax = (const u16*)d_in[0]; const u16* ckv = (const u16*)d_in[1];
    const u16* wq = (const u16*)d_in[2]; const u16* wuk = (const u16*)d_in[3]; const u16* wuv = (const u16*)d_in[4]; const u16* wo = (const u16*)d_in[5];
    const u16* Bx = (const u16*)d_in[6]; const u16* Lre = (const u16*)d_in[7]; const u16* Lim = (const u16*)d_in[8];
    const u16* Bre = (const u16*)d_in[9]; const u16* Bim = (const u16*)d_in[10]; const u16* Cre = (const u16*)d_in[11]; const u16* Cim = (const u16*)d_in[12];
    const u16* logdt = (const u16*)d_in[13]; const u16* Dsk = (const u16*)d_in[14];
    float* out = (float*)d_out;
    char* ws = (char*)d_ws;
    u16* Q = (u16*)ws; u16* K = (u16*)(ws + (16u << 20)); u16* V = (u16*)(ws + (32u << 20)); u16* ctx = (u16*)(ws + (48u << 20));
    float2* Bu = (float2*)(ws + (64u << 20));
    gemm_kn<<<dim3(16, 2048), 256, 0, stream>>>(Ax, wq, Q, 2048, 4096, 512);
    gemm_kn<<<dim3(16, 2048), 256, 0, stream>>>(ckv, wuk, K, 2048, 4096, 512);
    gemm_kn<<<dim3(16, 2048), 256, 0, stream>>>(ckv, wuv, V, 2048, 4096, 512);
    attn_naive<<<dim3(2048, 32), 128, 0, stream>>>(Q, K, V, ctx);
    gemm_out<<<dim3(2, 2048), 256, 0, stream>>>(ctx, wo, out, 2048, 512, 4096);
    bu_naive<<<dim3(2, 2048), 256, 0, stream>>>(Bx, Bre, Bim, Lre, Lim, logdt, Bu);
    scan_naive<<<dim3(8), 64, 0, stream>>>(Bu, Lre, Lim, logdt);
    y_naive<<<dim3(2, 2048), 256, 0, stream>>>(Bu, Cre, Cim, Bx, Dsk, out);
}
```

```cpp
#include <hip/hip_runtime.h>
#include <stdint.h>
#include <utility>

typedef unsigned short u16;
typedef short bf16x8 __attribute__((ext_vector_type(8)));
typedef short s16x4 __attribute__((ext_vector_type(4)));
typedef float f32x16 __attribute__((ext_vector_type(16)));
typedef float f32x4 __attribute__((ext_vector_type(4)));
typedef float f32x2 __attribute__((ext_vector_type(2)));
typedef __bf16 bf16x2_t __attribute__((ext_vector_type(2)));
typedef unsigned u32x4 __attribute__((ext_vector_type(4)));
#define LAS __attribute__((address_space(3)))
#define DI __device__ __forceinline__
#define MFMA(a, b, c) __builtin_amdgcn_mfma_f32_32x32x16_bf16((a), (b), (c), 0, 0, 0)
#define SBAR() __builtin_amdgcn_sched_barrier(0)
template <int... Is> using iseq = std::integer_sequence<int, Is...>;
template <int N> using make_iseq = std::make_integer_sequence<int, N>;

constexpr size_t WS_KV = 0;
constexpr size_t WS_CTX = 32u << 20;
constexpr size_t WS_WOF = 48u << 20;
constexpr size_t WS_HF = 52u << 20;
constexpr size_t WS_CF = 56u << 20;
constexpr size_t WS_HL = 58u << 20;
constexpr size_t WS_E = 66u << 20;
struct Params {
    const u16 *ax, *ckv, *wq, *wuk, *wuv, *wo, *bx, *lre, *lim, *bre, *bim, *cre, *cim, *logdt, *dsk;
    float* out; char* ws;
};
DI float bf2f(u16 v) { return __uint_as_float(((unsigned)v) << 16); }
struct cplx { float r, i; };
DI cplx cmul(cplx a, cplx b) { return {a.r * b.r - a.i * b.i, a.r * b.i + a.i * b.r}; }
DI cplx cfma(cplx a, cplx b, cplx c) { return {a.r * b.r - a.i * b.i + c.r, a.r * b.i + a.i * b.r + c.i}; }

DI unsigned cvtpk(float lo, float hi) { f32x2 v = {lo, hi}; bf16x2_t b = __builtin_convertvector(v, bf16x2_t); return __builtin_bit_cast(unsigned, b); }
DI u32x4 pack8(const f32x16& x, int s) {
    u32x4 p = {cvtpk(x[8 * s + 0], x[8 * s + 1]), cvtpk(x[8 * s + 2], x[8 * s + 3]), cvtpk(x[8 * s + 4], x[8 * s + 5]), cvtpk(x[8 * s + 6], x[8 * s + 7])};
    return p;
}
DI void glds16(unsigned voff, const void* sbase, unsigned ldsb) {
    unsigned keep;
    asm volatile("s_mov_b32 %0, m0\n\ts_mov_b32 m0, %3\n\ts_nop 0\n\tglobal_load_lds_dwordx4 %1, %2\n\ts_mov_b32 m0, %0" : "=&s"(keep) : "v"(voff), "s"(sbase), "s"(ldsb) : "memory");
}
DI void glds16x4(unsigned voff, const void* sbase, unsigned ldsb) {
    unsigned keep;
    asm volatile("s_mov_b32 %0, m0\n\ts_mov_b32 m0, %3\n\ts_nop 0\n\tglobal_load_lds_dwordx4 %1, %2\n\tglobal_load_lds_dwordx4 %1, %2 offset:1024\n\t"
                 "global_load_lds_dwordx4 %1, %2 offset:2048\n\tglobal_load_lds_dwordx4 %1, %2 offset:3072\n\ts_mov_b32 m0, %0" : "=&s"(keep) : "v"(voff), "s"(sbase), "s"(ldsb) : "memory");
}
template <int OFF> DI void glds16o(unsigned voff, const void* sbase, unsigned ldsb) {
    asm volatile("s_mov_b32 m0, %2\n\ts_nop 0\n\tglobal_load_lds_dwordx4 %0, %1 offset:%3" :: "v"(voff), "s"(sbase), "s"(ldsb), "i"(OFF) : "memory");
}
template <int OFF> DI void glds16n(unsigned voff, const void* sbase) {
    asm volatile("global_load_lds_dwordx4 %0, %1 offset:%2" :: "v"(voff), "s"(sbase), "i"(OFF) : "memory");
}
template <int N> DI void wait_bar() { asm volatile("s_waitcnt vmcnt(%0) lgkmcnt(0)\n\ts_barrier" :: "i"(N) : "memory"); }
DI void bar() { asm volatile("s_waitcnt lgkmcnt(0)\n\ts_barrier" ::: "memory"); }
DI cplx ssm_lambar(const Params& P, int p, cplx& lam) {
    lam = {bf2f(P.lre[p]), bf2f(P.lim[p])};
    const float dt = __uint_as_float(cvtpk(expf(bf2f(P.logdt[p])), 0.f) << 16);
    const float e = expf(dt * lam.r); float sn, cs; sincosf(dt * lam.i, &sn, &cs);
    return {e * cs, e * sn};
}
DI bf16x8 lds_b128(const LAS char* p) { return *(const LAS bf16x8*)p; }
typedef short v4i16_t __attribute__((ext_vector_type(4)));
DI s16x4 lds_tr(const LAS char* p) { return __builtin_bit_cast(s16x4, __builtin_amdgcn_ds_read_tr16_b64_v4i16((LAS v4i16_t*)p)); }
DI bf16x8 cat(s16x4 a, s16x4 b) { return __builtin_shufflevector(a, b, 0, 1, 2, 3, 4, 5, 6, 7); }

constexpr int S1_CRING = 0, S1_WRING = 65536, S1_LDS = 163840;
constexpr int s1_nst(int st) { return st == 8 ? 2 : st == 9 ? 2 : st == 10 ? 4 : st == 11 ? 2 : st == 12 ? 2 : st == 14 ? 2 : 0; }
constexpr int s1_wait(int st) {
    int tags[128] = {}; int n = 0;
    for (int i = 0; i < 4; ++i) tags[n++] = 100; for (int i = 0; i < 2; ++i) tags[n++] = 200;
    for (int i = 0; i < 4; ++i) tags[n++] = 101; for (int i = 0; i < 2; ++i) tags[n++] = 201; for (int i = 0; i < 2; ++i) tags[n++] = 202;
    for (int s = 0; s < st; ++s) {
        if (s + 2 < 8) for (int i = 0; i < 4; ++i) tags[n++] = 100 + s + 2;
        if (s + 3 < 16) for (int i = 0; i < 2; ++i) tags[n++] = 200 + s + 3;
        for (int i = 0; i < s1_nst(s); ++i) tags[n++] = 300;
    }
    int last = -1;
    for (int i = 0; i < n; ++i) if (tags[i] == 200 + st || (st < 8 && tags[i] == 100 + st)) last = i;
    return n - 1 - last;
}
#define RSTAMP(c, i) do {} while (0)
#define STAMP(c, i) do {} while (0)
struct S1 {
    unsigned* stamps; unsigned stv;
    bf16x8 wf[32];
    f32x16 acc[4];
    f32x16 accp[2];
    bf16x8 cf[6];
    unsigned voffc[2], voffw;
    const char* cbase;
    const char* wk;
    const char* wv;
    unsigned ldsb;
    unsigned w;
    const LAS char* crd[4];
    const LAS char* wrd;
    char* outp;
};
template <int U> DI void s1_dma_c(S1& c) {
    const unsigned dst = c.ldsb + S1_CRING + (U & 3) * 16384 + c.w * 2048;
    const unsigned mb = c.w >> 1, r0 = 16 * (c.w & 1);
    if constexpr (U < 8) {
        const char* b = c.cbase + (size_t)(32 * mb + r0) * 1024 + U * 128;
        glds16(c.voffc[0], b, dst); glds16(c.voffc[1], b + 8 * 1024, dst + 1024);
    } else {
        constexpr int v = U - 8;
        const char* b = c.cbase + (size_t)(128 + 32 * (v >> 1) + r0) * 1024 + (v & 1) * 512 + mb * 128;
        glds16(c.voffc[0], b, dst); glds16(c.voffc[1], b + 8 * 1024, dst + 1024);
    }
}
template <int ST> DI void s1_dma_w(S1& c) {
    const unsigned dst = c.ldsb + S1_WRING + (ST % 3) * 32768 + c.w * 4096;
    const char* bk = c.wk + (size_t)ST * 64 * 8192; const char* bv = c.wv + (size_t)ST * 64 * 8192;
    glds16(c.voffw, bk, dst); glds16(c.voffw, bk + 128, dst + 1024); glds16(c.voffw, bv, dst + 2048); glds16(c.voffw, bv + 128, dst + 3072);
}
template <int ST, int P> DI void s1_dma_w1(S1& c) {
    const unsigned dst = c.ldsb + S1_WRING + (ST % 3) * 32768 + c.w * 4096 + P * 1024;
    const char* bp = ((P < 2) ? c.wk : c.wv) + (size_t)ST * 64 * 8192 + (P & 1) * 128;
    glds16(c.voffw, bp, dst);
}
template <int U, int E> DI void s1_dma_c1(S1& c) {
    const unsigned dst = c.ldsb + S1_CRING + (U & 3) * 16384 + c.w * 2048 + E * 1024;
    const unsigned mb = c.w >> 1, r0 = 16 * (c.w & 1) + 8 * E;
    if constexpr (U < 8) glds16(c.voffc[E], c.cbase + (size_t)(32 * mb + r0) * 1024 + U * 128, dst);
    else { constexpr int v = U - 8; glds16(c.voffc[E], c.cbase + (size_t)(128 + 32 * (v >> 1) + r0) * 1024 + (v & 1) * 512 + mb * 128, dst); }
}
#define STMODE 2
DI void st16(char* p, u32x4 v) {
    asm volatile("global_store_dwordx4 %0, %1, off sc1\n\ts_nop 1" :: "v"(p), "v"(v) : "memory");
}
template <bool ISV> DI void s1_store(S1& c, const f32x16& a, int kblk) {
    char* p = c.outp + (kblk >> 1) * 32768 + (ISV ? (kblk & 1) * 2048 : (kblk & 1) * 8192);
    st16(p, pack8(a, 0)); st16(p + 1024, pack8(a, 1));
}
constexpr int S1_D = 4, S1_NB = 6;
template <bool ISV, int G> DI void s1_gap(S1& c) {
    constexpr int R = G + S1_D;
    if constexpr (R < 256) {
        constexpr int st = R >> 4, i = R & 15;
        if constexpr (i == 0) { STAMP(c, 2 * st + 1); wait_bar<s1_wait(st)>(); STAMP(c, 2 * st + 2); RSTAMP(c, 44 + st); }
        constexpr int cs = S1_CRING + (st & 3) * 16384;
        constexpr int s = st < 8 ? (i >> 2) : (i & 3), mb = st < 8 ? (i & 3) : (i >> 2);
        if constexpr (st < 8 && (i & 3) == 0) { constexpr int wsl = (st % 3) * 32768 + s * 8192; c.wf[R >> 2] = cat(lds_tr(c.wrd + wsl), lds_tr(c.wrd + wsl + 256)); }
        c.cf[R % S1_NB] = lds_b128(c.crd[s] + cs + mb * 4096);
    }
    if constexpr (G >= 0) {
        constexpr int st = G >> 4, i = G & 15;
        if constexpr (st < 8) {
            constexpr int s = i >> 2, mb = i & 3, ks = 4 * st + s;
            if constexpr (ks == 0) c.acc[mb] = f32x16{};
            c.acc[mb] = ISV ? MFMA(c.cf[G % S1_NB], c.wf[ks], c.acc[mb]) : MFMA(c.wf[ks], c.cf[G % S1_NB], c.acc[mb]);
        } else {
            constexpr int u = st - 8, ai = (u >> 1) & 1, ks = 16 * (u & 1) + i;
            if constexpr (ks == 0) c.accp[ai] = f32x16{};
            c.accp[ai] = ISV ? MFMA(c.cf[G % S1_NB], c.wf[ks], c.accp[ai]) : MFMA(c.wf[ks], c.cf[G % S1_NB], c.accp[ai]);
        }
    }
    if constexpr (R < 256) {
        constexpr int st = R >> 4, i = R & 15;
        if constexpr (st + 2 < 8) {
            if constexpr (i == 1) s1_dma_w1<st + 2, 0>(c); if constexpr (i == 3) s1_dma_w1<st + 2, 1>(c);
            if constexpr (i == 5) s1_dma_w1<st + 2, 2>(c); if constexpr (i == 7) s1_dma_w1<st + 2, 3>(c);
            if constexpr (i == 9) s1_dma_c1<st + 3, 0>(c); if constexpr (i == 11) s1_dma_c1<st + 3, 1>(c);
        } else if constexpr (st + 3 < 16) {
            if constexpr (i == 1) s1_dma_c1<st + 3, 0>(c); if constexpr (i == 4) s1_dma_c1<st + 3, 1>(c);
        }
        if constexpr (st == 8 && i == 10) s1_store<ISV>(c, c.acc[0], 0);
        if constexpr (st == 9 && i == 10) s1_store<ISV>(c, c.acc[1], 1);
        if constexpr (st == 10 && i == 7) s1_store<ISV>(c, c.acc[2], 2);
        if constexpr (st == 10 && i == 12) s1_store<ISV>(c, c.accp[0], 4);
        if constexpr (st == 11 && i == 10) s1_store<ISV>(c, c.acc[3], 3);
        if constexpr (st == 12 && i == 10) s1_store<ISV>(c, c.accp[1], 5);
        if constexpr (st == 14 && i == 10) s1_store<ISV>(c, c.accp[0], 6);
    }
    if constexpr (G == 255) s1_store<ISV>(c, c.accp[1], 7);
    SBAR();
}
template <bool ISV, int... Gs> DI void s1_all(S1& c, iseq<Gs...>) { (s1_gap<ISV, Gs - S1_D>(c), ...); }

template <int Q> DI void xb_dma(S1& c, const char* xsrc, const char* bsrc) {
    const unsigned sl = c.ldsb + (Q % 3) * 49152;
#pragma unroll
    for (int u = 0; u < 2; ++u)
#pragma unroll
        for (int e = 0; e < 2; ++e) glds16o<0>(c.voffc[e], xsrc + Q * 256 + u * 128 + e * 8192, sl + u * 16384 + c.w * 2048 + e * 1024);
#pragma unroll
    for (int e = 0; e < 2; ++e) glds16o<0>(c.voffc[e], bsrc + Q * 256 + e * 8192, sl + 32768 + c.w * 2048 + e * 1024);
}
template <int Q> DI void xb_stage(S1& c, f32x16& acc, const char* xsrc, const char* bsrc, unsigned aoff, unsigned boff) {
    wait_bar<(Q == 0 ? 12 : Q == 3 ? 0 : 6)>();
    if constexpr (Q == 1) xb_dma<3>(c, xsrc, bsrc);
    constexpr int sl = (Q % 3) * 49152;
#pragma unroll
    for (int u = 0; u < 2; ++u)
#pragma unroll
        for (int s = 0; s < 4; ++s) acc = MFMA(lds_b128(c.crd[s] + sl + u * 16384 + aoff), lds_b128(c.crd[s] + sl + 32768 + u * 8192 + boff), acc);
}

__global__ __launch_bounds__(512, 2) void k_stage1(const Params P) {
    extern __shared__ __attribute__((aligned(16))) char lds[];
    char* ws = P.ws;
    const unsigned tid = threadIdx.x, l = tid & 63, w = __builtin_amdgcn_readfirstlane(tid >> 6);
    const unsigned b = blockIdx.x, xcd = b & 7, idx = b >> 3, h = 4 * xcd + (idx & 3), kb = idx >> 2;
    const unsigned r = l & 31, hi = l >> 5;
    S1 c;
    c.w = w; c.ldsb = (unsigned)(uintptr_t)(LAS char*)lds;
    c.cbase = (const char*)P.ckv + (size_t)kb * 256 * 1024;
    c.wk = (const char*)P.wuk + h * 256 + (size_t)w * 8 * 8192; c.wv = (const char*)P.wuv + h * 256 + (size_t)w * 8 * 8192;
#pragma unroll
    for (int e = 0; e < 2; ++e) c.voffc[e] = (l >> 3) * 1024 + (((l & 7) ^ ((4 * e + (l >> 4)) & 7)) * 16);
    c.voffw = ((l >> 2) & 7) * 8192 + (l >> 5) * 64 + (l & 3) * 16;
    const LAS char* L = (const LAS char*)lds;
#pragma unroll
    for (int s = 0; s < 4; ++s) c.crd[s] = L + r * 128 + (((2 * s + hi) ^ ((r >> 1) & 7)) * 16);
    c.wrd = L + S1_WRING + w * 512 + hi * 4096 + ((l >> 2) & 3) * 64 + ((l >> 4) & 1) * 32 + (l & 3) * 8;
    c.outp = ws + WS_KV + ((size_t)h * 32 + 4 * kb) * 32768 + l * 16 + (w < 4 ? w * 2048 : 16384 + (w - 4) * 4096);
    c.stamps = (unsigned*)(ws + (128u << 20)) + ((size_t)b * 8 + w) * 64; c.stv = 0;
    STAMP(c, 0); RSTAMP(c, 40);
    {
        const unsigned ch = 2 * xcd + (idx >> 4), gq = idx & 15;
        { const unsigned vo = ((l >> 2) & 7) * 1024 + (l >> 5) * 64 + (l & 3) * 16; const char* src = (const char*)P.wo + (size_t)b * 16384 + w * 128;
          glds16o<0>(vo, src, c.ldsb + 147456 + w * 2048); glds16o<0>(vo, src + 8192, c.ldsb + 147456 + w * 2048 + 1024); }
        const char* xsrc = (const char*)P.bx + (size_t)(128 * ch + 16 * w) * 1024;
        const char* bsrc = (const char*)(((w >> 1) & 1) ? P.bim : P.bre) + (size_t)(32 * gq + 16 * (w & 1)) * 1024 + (w >> 2) * 128;
        xb_dma<0>(c, xsrc, bsrc); xb_dma<1>(c, xsrc, bsrc); xb_dma<2>(c, xsrc, bsrc);
        if (w < 4) {
            const unsigned part = w >> 1, n = 8 * (b >> 2) + (l & 7), chk = 16 * (b & 3) + 8 * (w & 1) + (l >> 3);
            const u32x4 v = *(const u32x4*)((const char*)(part ? P.cim : P.cre) + (size_t)n * 1024 + chk * 16);
            st16(ws + WS_CF + ((size_t)(n >> 5) * 64 + part * 32 + (chk >> 1)) * 1024 + ((n & 31) + 32 * (chk & 1)) * 16, v);
        }
        const int pl = tid & 31, sc = tid >> 5, pg = 32 * gq + pl;
        cplx lam; const cplx lb = ssm_lambar(P, pg, lam);
        const float den = 1.f / (lam.r * lam.r + lam.i * lam.i);
        const cplx coef = {((lb.r - 1.f) * lam.r + lb.i * lam.i) * den, (lb.i * lam.r - (lb.r - 1.f) * lam.i) * den};
        asm volatile("s_waitcnt vmcnt(18)" ::: "memory");
        { const LAS char* tp = L + 147456 + w * 2048 + (4 * hi + ((l >> 2) & 3)) * 64 + ((l >> 4) & 1) * 32 + (l & 3) * 8;
#pragma unroll
          for (int nb = 0; nb < 2; ++nb) st16(ws + WS_WOF + ((size_t)(2 * w + nb) * 256 + b) * 1024 + l * 16, __builtin_bit_cast(u32x4, cat(lds_tr(tp + nb * 512), lds_tr(tp + nb * 512 + 1024)))); }
        f32x16 acc = {};
        const unsigned aoff = (w & 3) * 4096, boff = (w >> 2) * 4096;
        xb_stage<0>(c, acc, xsrc, bsrc, aoff, boff); xb_stage<1>(c, acc, xsrc, bsrc, aoff, boff); xb_stage<2>(c, acc, xsrc, bsrc, aoff, boff); xb_stage<3>(c, acc, xsrc, bsrc, aoff, boff);
        LAS float* U = (LAS float*)((LAS char*)lds + 131072);
#pragma unroll
        for (int i = 0; i < 16; ++i) U[(w >> 2) * 4096 + (32 * (w & 3) + (i & 3) + 8 * (i >> 2) + 4 * hi) * 32 + r] = acc[i];
        bar();
        s1_dma_w<0>(c); s1_dma_c<0>(c); s1_dma_w<1>(c); s1_dma_c<1>(c); s1_dma_c<2>(c);
        cplx hh[8]; cplx hs = {0.f, 0.f};
#pragma unroll
        for (int i = 0; i < 8; ++i) { const cplx u = {U[(8 * sc + i) * 32 + pl], U[4096 + (8 * sc + i) * 32 + pl]}; hs = cfma(lb, hs, cmul(coef, u)); hh[i] = hs; }
        LAS f32x2* E2 = (LAS f32x2*)((LAS char*)lds + 49152);
        E2[sc * 32 + pl] = (f32x2){hs.r, hs.i};
        cplx l8 = cmul(lb, lb); l8 = cmul(l8, l8); l8 = cmul(l8, l8);
        bar();
        cplx cy = {0.f, 0.f};
#pragma unroll
        for (int s2 = 0; s2 < 15; ++s2) { const f32x2 e = E2[s2 * 32 + pl]; const cplx nx = cfma(l8, cy, cplx{e[0], e[1]}); if (s2 < sc) cy = nx; }
        f32x2* HL = (f32x2*)(ws + WS_HL) + (size_t)(128 * ch + 8 * sc) * 512 + pg;
#pragma unroll
        for (int i = 0; i < 8; ++i) { cy = cmul(lb, cy); HL[i * 512] = (f32x2){hh[i].r + cy.r, hh[i].i + cy.i}; }
        if (sc == 15) ((f32x2*)(ws + WS_E))[ch * 512 + pg] = (f32x2){hh[7].r + cy.r, hh[7].i + cy.i};
        SBAR();
    }
    STAMP(c, 35);
    if (w < 4) s1_all<false>(c, make_iseq<256 + S1_D>{}); else s1_all<true>(c, make_iseq<256 + S1_D>{});
    STAMP(c, 33);
}

#define PRIO_MM 1
#define PRIO_V 0
#define PIN(x) asm volatile("" : "+v"(x))
#define PINS(x) asm volatile("" : "+s"(x))
#define MX3(a, b, c) __builtin_fmaxf(__builtin_fmaxf((a), (b)), (c))
constexpr float QSCALE = 0.08838834764831845f * 1.4426950408889634f;
constexpr int A_LDS = 163840;
constexpr int QP_SLOT = 49152;
constexpr int A_Z = 49152, A_ZB = 98304, A_ML = 81920;
DI unsigned kaddr(int t) { return 98304u + (unsigned)((t + 3) & 3) * 16384u; }
DI unsigned vaddr(int t) { return (unsigned)(t - 3 * ((t * 11) >> 5)) * 16384u; }
template <bool FIRST, int OFF> DI void glds16c(unsigned voff, const void* sbase, unsigned ldsb, unsigned long long em) {
    if constexpr (FIRST) asm volatile("s_mov_b32 m0, %2\n\ts_mov_b64 exec, %3\n\tglobal_load_lds_dwordx4 %0, %1\n\ts_mov_b64 exec, -1" :: "v"(voff), "s"(sbase), "s"(ldsb), "s"(em) : "memory");
    else asm volatile("s_mov_b64 exec, %2\n\tglobal_load_lds_dwordx4 %0, %1 offset:%3\n\ts_mov_b64 exec, -1" :: "v"(voff), "s"(sbase), "s"(em), "i"(OFF) : "memory");
}

struct AT {
    bf16x8 qf[8];
    f32x16 o[4];
    f32x16 s[2];
    f32x16 negm;
    u32x4 pf[4];
    bf16x8 fb[8];
    float m, l, rm, a[4];
    bf16x8 ones;
    bool lo16;
    const char* ks; const char* vs; unsigned kd, vd, special, t0, t1; unsigned long long km, vm; int p;
    unsigned tab0, tab1;
    unsigned ldsb, ldsr, w, g, rg, lane16;
    const char* kvsrc;
    const LAS char* L;
    const LAS char* kb;
    const LAS char* vb0; const LAS char* vb1;
    char* ctx;
    int n, rhi, rb1, rb2;
    unsigned* stamps; unsigned stv; unsigned wv, wb, nres; unsigned seg[12]; unsigned long long tl;
};
#define SEG(c, i) do {} while (0)
constexpr int AD = 5;
template <int F> DI void at_rd(AT& c) {
    if constexpr (F < 16) c.fb[F & 7] = lds_b128(c.kb + ((F & 1) * 8 + (F >> 1)) * 1024);
    else if constexpr (F < 32) { constexpr int ks = (F - 16) >> 2, dt = (F - 16) & 3; c.fb[F & 7] = lds_b128((dt < 2 ? c.vb0 : c.vb1) + ((dt & 1) * 4 + ks) * 1024); }
}
DI void at_tables(AT& c, int lane) {
    const int p = lane - 1, n = c.n, g = (int)c.g, y = 2 * p + g + 2;
    const int tr_k = p + 1 < n ? p + 1 : 2 * (p + 1) - n + g, tr_v = p < n ? p : 2 * p - n + g;
    const int tk = y >= 2 * n - 3 ? y + 3 - n : (g ? (y + 3) >> 1 : 99), tv = y >= 2 * n ? y - n : (g ? 99 : y >> 1);
    c.tab0 = kaddr(tr_k) | (tk < 34 - n ? 1u : 0u) | ((unsigned)(tk & 31) << 20) | (kaddr(tk) >> 10);
    c.tab1 = (vaddr(tr_v & 31) + g * 8192u) | (tv < 34 - n ? 1u : 0u) | ((unsigned)(tv & 31) << 20) | (vaddr(tv & 31) >> 10);
}
template <int J> DI void at_setup(AT& c) {
    if constexpr (J == 0) { c.p += 1; PINS(c.p); c.t0 = __builtin_amdgcn_readlane(c.tab0, c.p + 1); c.t1 = __builtin_amdgcn_readlane(c.tab1, c.p + 1); PINS(c.t0); PINS(c.t1); }
    if constexpr (J == 2) { c.kb = c.L + c.lane16 + (c.t0 & 0x3c000u); PIN(c.kb); }
    if constexpr (J == 3) { c.kd = c.ldsr + ((c.t0 & 0xf0u) << 10); PINS(c.kd); }
    if constexpr (J == 4) { c.vd = c.ldsr + ((c.t1 & 0xf0u) << 10); PINS(c.vd); }
    if constexpr (J == 5) { c.km = (c.t0 & 1u) ? ~0ull : 0ull; PINS(c.km); }
    if constexpr (J == 6) { c.vm = (c.t1 & 1u) ? ~0ull : 0ull; PINS(c.vm); }
    if constexpr (J == 7) { c.ks = c.kvsrc + ((c.t0 >> 20) << 15); PINS(c.ks); }
    if constexpr (J == 8) { c.vs = c.kvsrc + (((c.t1 >> 20) << 15) + 16384u); PINS(c.vs); }
    if constexpr (J == 12) { c.vb0 = c.L + c.lane16 + (c.t1 & 0x3e000u); PIN(c.vb0); }
    if constexpr (J == 13) { c.vb1 = c.L + c.lane16 + ((c.t1 & 0x3e000u) ^ 8192u); PIN(c.vb1); }
}
template <int... Js> DI void at_setup_all(AT& c, iseq<Js...>) { (at_setup<Js>(c), ...); }
template <bool RDV, int F> DI void mm_gap(AT& c) {
    if constexpr (F + AD < 16 || RDV) at_rd<F + AD>(c);
    if constexpr (F < 16) {
        constexpr int kb2 = F & 1, d0 = F >> 1;
        if constexpr (d0 == 0) asm volatile("v_mfma_f32_32x32x16_bf16 %0, %1, %2, %3" : "=&v"(c.s[kb2]) : "v"(c.fb[F & 7]), "v"(c.qf[0]), "v"(c.negm));
        else c.s[kb2] = MFMA(c.fb[F & 7], c.qf[d0], c.s[kb2]);
        PIN(c.s[kb2]);
        if constexpr ((F & 1) == 0) { if constexpr (F < 8) glds16c<F == 0, (F >> 1) * 1024>(c.lane16, c.vs, c.vd, c.vm); else glds16c<F == 8, ((F - 8) >> 1) * 1024>(c.lane16, c.ks, c.kd, c.km); }
    } else {
        constexpr int ks = (F - 16) >> 2, dt = (F - 16) & 3;
        c.o[dt] = MFMA(c.fb[F & 7], __builtin_bit_cast(bf16x8, c.pf[ks]), c.o[dt]); PIN(c.o[dt]);
        at_setup<F - 16>(c);
        if constexpr (F == 28) bar();
    }
    SBAR();
}
template <bool RDV, int B, int... Fs> DI void mm_gaps(AT& c, iseq<Fs...>) { (mm_gap<RDV, B + Fs>(c), ...); }
template <int B, int... Fs> DI void pre_rd(AT& c, iseq<Fs...>) { (at_rd<B + Fs>(c), ...); }
DI void at_fix(AT& c, bool first) {
    float a0 = MX3(c.s[0][0], c.s[0][1], c.s[0][2]), a1 = MX3(c.s[0][8], c.s[0][9], c.s[0][10]), a2 = MX3(c.s[1][0], c.s[1][1], c.s[1][2]), a3 = MX3(c.s[1][8], c.s[1][9], c.s[1][10]);
    a0 = MX3(a0, c.s[0][3], c.s[0][4]); a1 = MX3(a1, c.s[0][11], c.s[0][12]); a2 = MX3(a2, c.s[1][3], c.s[1][4]); a3 = MX3(a3, c.s[1][11], c.s[1][12]);
    a0 = MX3(a0, c.s[0][5], c.s[0][6]); a1 = MX3(a1, c.s[0][13], c.s[0][14]); a2 = MX3(a2, c.s[1][5], c.s[1][6]); a3 = MX3(a3, c.s[1][13], c.s[1][14]);
    a0 = MX3(a0, c.s[0][7], c.s[0][15]); a2 = MX3(a2, c.s[1][7], c.s[1][15]); a0 = MX3(a0, a1, a2); a0 = __builtin_fmaxf(a0, a3);
    auto rr = __builtin_amdgcn_permlane32_swap(__float_as_uint(a0), __float_as_uint(a0), false, false);
    const float rm = __builtin_fmaxf(__uint_as_float(rr[0]), __uint_as_float(rr[1]));
    const float dl = __builtin_fmaxf(rm, first ? -1e30f : 0.f);
    c.m += dl;
#pragma unroll
    for (int r = 0; r < 16; ++r) { c.s[0][r] -= dl; c.s[1][r] -= dl; c.negm[r] = -c.m; }
    if (!first) {
        const float f = __builtin_amdgcn_exp2f(-dl); c.l *= f;
#pragma unroll
        for (int dt = 0; dt < 4; ++dt) c.o[dt] *= f;
        c.nres++;
    }
}
DI void at_mask(AT& c, int kbase) {
#pragma unroll
    for (int kb2 = 0; kb2 < 2; ++kb2) {
        const int d = kbase + kb2 - (int)c.rg;
        if (d >= 0) {
            const unsigned long long full = d > 0 ? ~0ull : 0ull;
#pragma unroll
            for (int r = 0; r < 16; ++r) {
                const int cc = (r & 3) + 8 * (r >> 2);
                unsigned long long mk = (((1ull << cc) - 1) | (((1ull << (cc + 4)) - 1) << 32)) | full;
                asm volatile("v_cndmask_b32_e64 %0, %0, %1, %2" : "+v"(c.s[kb2][r]) : "v"(-INFINITY), "s"(mk));
            }
        }
    }
}
DI void at_vphase(AT& c) {
    const int p = c.p;
    pre_rd<0>(c, make_iseq<AD>{});
    SBAR();
    bool fix = false, fin = false;
    if (__builtin_expect((c.special >> p) & 1, 0)) {
        fin = c.g == 0 && p == c.n;
        if (fin) {
            unsigned l16 = c.lane16; PIN(l16);
            asm volatile("s_waitcnt vmcnt(0)" ::: "memory");
            const float rl = __builtin_amdgcn_rcpf(c.rm);
            char* op = c.ctx + (size_t)c.rb1 * 262144 + l16;
#pragma unroll
            for (int dt = 0; dt < 4; ++dt) { const f32x16 v = c.o[dt] * rl; st16(op + dt * 2048, pack8(v, 0)); st16(op + dt * 2048 + 1024, pack8(v, 1)); c.o[dt] = f32x16{}; }
            SBAR();
        }
        at_mask(c, p == 16 ? 2 * (int)c.g : (c.g == 0 && p < c.n && p >= c.n - 2) ? 2 * (p - c.n + 2) : -8);
        fix = p == 0 || fin;
    }
    SEG(c, 0);
    const bool first = fix;
    float own;
    for (;;) {
        if (__builtin_expect(fix, 0)) at_fix(c, first);
#pragma unroll
        for (int k = 0; k < 4; ++k) {
#pragma unroll
            for (int j = 0; j < 4; ++j)
                c.pf[k][j] = cvtpk(__builtin_amdgcn_exp2f(c.s[k >> 1][8 * (k & 1) + 2 * j]), __builtin_amdgcn_exp2f(c.s[k >> 1][8 * (k & 1) + 2 * j + 1]));
            PIN(c.pf[k]);
        }
        SBAR();
        SEG(c, 4);
        __builtin_amdgcn_s_setprio(3);
        f32x4 lt = {0.f, 0.f, 0.f, 0.f};
#pragma unroll
        for (int k = 0; k < 4; ++k) lt = __builtin_amdgcn_mfma_f32_16x16x32_bf16(c.ones, __builtin_bit_cast(bf16x8, c.pf[k]), lt, 0, 0, 0);
        __builtin_amdgcn_s_setprio(PRIO_V);
        own = c.lo16 ? lt[0] : lt[1];
        if (__builtin_expect(fix || !__any(!(own <= 1.0995116e12f)), 1)) break;
        fix = true;
    }
    c.l += own;
    if (__builtin_expect(c.g == 0 && p == c.n - 1, 0)) {
#pragma unroll
        for (int i = 0; i < 8; ++i) c.qf[i] = lds_b128(c.L + A_Z + c.rg * 8192 + c.lane16 + i * 1024);
        c.m = 0.f; c.negm = f32x16{}; c.rm = c.l; c.l = 0.f;
    }
    PIN(c.negm);
    SEG(c, 5);
    if (!fin) asm volatile("s_waitcnt vmcnt(0)" ::: "memory");
    bar();
    SEG(c, 6);
}

struct QP {
    f32x16 acc[4];
    bf16x8 xf[2], wf[4];
    unsigned voffx[2], voffw;
    const char* xsrc;
    const char* wsrc;
    const LAS char* xrd[4];
    const LAS char* wrd;
};
template <int ST, int P> DI void qp_dma1(AT& c, QP& q) {
    const unsigned sl = c.ldsb + (ST % 3) * QP_SLOT;
    if constexpr (P < 2) glds16o<0>(q.voffw, q.wsrc + (size_t)ST * 64 * 8192 + P * 128, sl + c.w * 2048 + P * 1024);
    else glds16o<0>(q.voffx[P & 1], q.xsrc + ST * 128 + (P - 2) * 8192, sl + 16384 + c.w * 4096 + (P - 2) * 1024);
}
template <int ST, int... Ps> DI void qp_dma(AT& c, QP& q, iseq<Ps...>) { (qp_dma1<ST, Ps>(c, q), ...); }
constexpr int QD = 3;
template <int G> DI void qp_gap(AT& c, QP& q) {
    constexpr int R = G + QD;
    if constexpr (R < 128) {
        constexpr int st = R >> 4, i = R & 15, s = i >> 2, dt = i & 3, sl = (st % 3) * QP_SLOT;
        if constexpr (i == 0) wait_bar<(st < 7 ? 6 : 2)>();
        if constexpr (dt == 0) q.xf[s & 1] = lds_b128(q.xrd[s] + sl);
        q.wf[R & 3] = cat(lds_tr(q.wrd + sl + s * 4096 + dt * 512), lds_tr(q.wrd + sl + s * 4096 + dt * 512 + 256));
    }
    if constexpr (G >= 0) {
        constexpr int s = (G & 15) >> 2, dt = G & 3;
        if constexpr (G < 4) q.acc[dt] = f32x16{};
        q.acc[dt] = MFMA(q.wf[G & 3], q.xf[s & 1], q.acc[dt]);
    }
    if constexpr (R < 128) {
        constexpr int st = R >> 4, i = R & 15;
        if constexpr (st + 2 < 8 && (i & 1) == 1 && i < 12) qp_dma1<st + 2, (i >> 1)>(c, q);
        if constexpr (st == 6 && (i == 1 || i == 3)) {
            glds16o<0>(c.lane16, c.kvsrc - c.rg * 4096 + c.w * 2048 + 32768 + (i >> 1) * 1024, c.ldsb + kaddr(1) + c.w * 2048 + (i >> 1) * 1024);
        }
    }
    SBAR();
}
template <int... Gs> DI void qp_all(AT& c, QP& q, iseq<Gs...>) { (qp_gap<Gs - QD>(c, q), ...); }

__global__ __launch_bounds__(512, 2) void k_attn(const Params P) {
    extern __shared__ __attribute__((aligned(16))) char lds[];
    char* ws = P.ws; const u16* x = P.ax; const u16* wq = P.wq;
    const unsigned tid = threadIdx.x, l = tid & 63, w = __builtin_amdgcn_readfirstlane(tid >> 6);
    const unsigned b = blockIdx.x, xcd = b & 7, idx = b >> 3, h = 4 * xcd + (idx & 3), pi = idx >> 2;
    const unsigned r = l & 31, hi = l >> 5, g = w >> 2, rg = w & 3;
    const int rb1 = 4 * (int)pi + (int)rg, rb2 = 4 * (15 - (int)pi) + (int)rg;
    AT c;
    c.w = w; c.g = g; c.rg = rg; c.ldsb = (unsigned)(uintptr_t)(LAS char*)lds; c.L = (const LAS char*)lds; c.lane16 = l * 16; c.ldsr = c.ldsb + rg * 4096;
    c.kvsrc = ws + WS_KV + (size_t)h * 32 * 32768 + rg * 4096;
    c.ctx = ws + WS_CTX + (size_t)h * 8192;
    c.rb1 = rb1; c.rb2 = rb2;
    c.n = 2 * (int)pi + 2; c.rhi = (int)r - 4 * (int)hi;
    c.stamps = (unsigned*)(ws + (128u << 20)) + ((size_t)b * 8 + w) * 64; c.stv = 0; c.wv = 0; c.wb = 0; c.nres = 0;
    STAMP(c, 0); RSTAMP(c, 60);
    {
        QP q;
        q.xsrc = (const char*)x + (size_t)(g ? rb2 : rb1) * 32768; q.wsrc = (const char*)wq + h * 256 + (size_t)w * 8 * 8192;
#pragma unroll
        for (int e = 0; e < 2; ++e) q.voffx[e] = (l >> 3) * 1024 + (((l & 7) ^ ((4 * e + (l >> 4)) & 7)) * 16);
        q.voffw = ((l >> 2) & 7) * 8192 + (l >> 5) * 64 + (l & 3) * 16;
#pragma unroll
        for (int s = 0; s < 4; ++s) q.xrd[s] = c.L + 16384 + w * 4096 + r * 128 + (((2 * s + hi) ^ ((r >> 1) & 7)) * 16);
        q.wrd = c.L + hi * 2048 + ((l >> 2) & 3) * 64 + ((l >> 4) & 1) * 32 + (l & 3) * 8;
        qp_dma<0>(c, q, make_iseq<6>{});
        { const char* src = c.kvsrc - rg * 4096 + w * 2048;
          glds16o<0>(c.lane16, src, c.ldsb + kaddr(0) + w * 2048); glds16n<1024>(c.lane16, src); }
        qp_dma<1>(c, q, make_iseq<6>{});
        {
            const unsigned ch = 2 * xcd + (idx >> 4), gq = idx & 15;
            const int pl = tid & 31, sc = tid >> 5, pg = 32 * gq + pl;
            const f32x2* HL = (const f32x2*)(ws + WS_HL) + (size_t)(128 * ch + 8 * sc) * 512 + pg;
            f32x2 hv[8], ev[15];
#pragma unroll
            for (int i = 0; i < 8; ++i) hv[i] = HL[i * 512];
#pragma unroll
            for (int i = 0; i < 15; ++i) ev[i] = ((const f32x2*)(ws + WS_E))[i * 512 + pg];
            cplx lam; const cplx lb = ssm_lambar(P, pg, lam);
            cplx pw = cmul(lb, lb); pw = cmul(pw, pw); pw = cmul(pw, pw);
            cplx ps = {1.f, 0.f};
#pragma unroll
            for (int bit = 0; bit < 4; ++bit) { const cplx t = cmul(ps, pw); if ((sc >> bit) & 1) ps = t; pw = cmul(pw, pw); }
            cplx hin = {0.f, 0.f};
#pragma unroll
            for (int i = 0; i < 15; ++i) if (i < (int)ch) hin = cfma(pw, hin, cplx{ev[i][0], ev[i][1]});
            cplx gg = cmul(ps, hin);
            LAS u16* T = (LAS u16*)((LAS char*)lds + 98304);
#pragma unroll
            for (int i = 0; i < 8; ++i) { gg = cmul(lb, gg); const unsigned pk = cvtpk(2.f * (hv[i][0] + gg.r), -2.f * (hv[i][1] + gg.i));
                T[(8 * sc + i) * 32 + pl] = (u16)pk; T[4096 + (8 * sc + i) * 32 + pl] = (u16)(pk >> 16); }
            bar();
#pragma unroll
            for (int j = 0; j < 2; ++j) {
                const unsigned t = tid & 127, qq = tid >> 7;
                const u32x4 v = *(const LAS u32x4*)((const LAS char*)T + j * 8192 + t * 64 + qq * 16);
                st16(ws + WS_HF + ((size_t)(4 * ch + (t >> 5)) * 64 + j * 32 + 2 * gq + (qq >> 1)) * 1024 + ((t & 31) + 32 * (qq & 1)) * 16, v);
            }
            SBAR();
        }
#pragma unroll
    for (int dt = 0; dt < 4; ++dt) { c.o[dt] = f32x16{}; PIN(c.o[dt]); }
    c.negm = f32x16{}; PIN(c.negm); c.m = 0.f; c.l = 0.f; c.rm = 1.f;
    { const short one = (((l >> 4) ^ l) & 1) ? (short)0 : (short)0x3F80; c.ones = (bf16x8){one, one, one, one, one, one, one, one}; PIN(c.ones); c.lo16 = (l & 16) == 0; }
    c.special = 1u | (1u << 16) | (g ? 0u : (7u << (2 * pi)));
    at_tables(c, (int)l & 31);
    c.p = -2; at_setup_all(c, make_iseq<16>{});
        SBAR();
        qp_all(c, q, make_iseq<128 + QD>{});
#pragma unroll
        for (int dt = 0; dt < 4; ++dt) {
            f32x16 v = q.acc[dt] * QSCALE;
            c.qf[2 * dt] = __builtin_bit_cast(bf16x8, pack8(v, 0)); c.qf[2 * dt + 1] = __builtin_bit_cast(bf16x8, pack8(v, 1));
        }
    }
    STAMP(c, 1); RSTAMP(c, 61);
    wait_bar<0>();
    if (g) {
        LAS char* qp_ = (LAS char*)c.L + A_Z + rg * 8192 + c.lane16;
#pragma unroll
        for (int i = 0; i < 8; ++i) *(LAS bf16x8*)(qp_ + i * 1024) = c.qf[i];
        pre_rd<0>(c, make_iseq<AD>{});
        bar();
    } else pre_rd<0>(c, make_iseq<AD>{});
    {
        __builtin_amdgcn_s_setprio(PRIO_MM);
        mm_gaps<false, 0>(c, make_iseq<16>{});
        __builtin_amdgcn_s_setprio(PRIO_V);
        at_setup_all(c, make_iseq<16>{});
        bar();
    }
    {
        for (int p = 0; p <= 16; ++p) {
            at_vphase(c);
            __builtin_amdgcn_s_setprio(PRIO_MM);
            mm_gaps<true, 0>(c, make_iseq<32>{});
            __builtin_amdgcn_s_setprio(PRIO_V);
            SEG(c, 7);
        }
    }
    STAMP(c, 2);
    {
        LAS char* z = (LAS char*)c.L + (g ? A_ZB : A_Z) + rg * 8192 + c.lane16;
#pragma unroll
        for (int i = 0; i < 4; ++i) { *(LAS f32x4*)(z + i * 1024) = (f32x4){c.o[2][4 * i], c.o[2][4 * i + 1], c.o[2][4 * i + 2], c.o[2][4 * i + 3]};
                                      *(LAS f32x4*)(z + 4096 + i * 1024) = (f32x4){c.o[3][4 * i], c.o[3][4 * i + 1], c.o[3][4 * i + 2], c.o[3][4 * i + 3]}; }
        *(LAS f32x2*)((LAS char*)c.L + A_ML + g * 2048 + rg * 512 + l * 8) = (f32x2){c.m, c.l};
        if (g == 0) bar();
        bar();
        const f32x2 ml = *(const LAS f32x2*)(c.L + A_ML + (1 - g) * 2048 + rg * 512 + l * 8);
        const float mm = __builtin_fmaxf(c.m, ml[0]);
        const float f0 = __builtin_amdgcn_exp2f(c.m - mm), f1 = __builtin_amdgcn_exp2f(ml[0] - mm);
        const float rl = __builtin_amdgcn_rcpf(c.l * f0 + ml[1] * f1);
        const float w0 = f0 * rl, w1 = f1 * rl;
        const LAS char* zr = c.L + (g ? A_Z : A_ZB) + rg * 8192 + c.lane16;
        char* op = c.ctx + (size_t)rb2 * 262144 + g * 4096 + l * 16;
#pragma unroll
        for (int t2 = 0; t2 < 2; ++t2) {
            f32x16 ra;
#pragma unroll
            for (int i = 0; i < 4; ++i) { const f32x4 pa = *(const LAS f32x4*)(zr + t2 * 4096 + i * 1024);
#pragma unroll
                for (int j = 0; j < 4; ++j) ra[4 * i + j] = c.o[t2][4 * i + j] * w0 + pa[j] * w1; }
            st16(op + t2 * 2048, pack8(ra, 0)); st16(op + t2 * 2048 + 1024, pack8(ra, 1));
        }
    }
    STAMP(c, 3); STAMP(c, 4); RSTAMP(c, 62);
}

constexpr int O_LDS = 131072, OD = 8;
struct OG { bf16x8 a[OD][2], b[OD][2]; f32x16 acc[4]; unsigned lane16; const char *a0, *b0; unsigned as, bs; };
template <int OFF> DI void gld(bf16x8& d, unsigned voff, const void* sbase) { asm volatile("global_load_dwordx4 %0, %1, %2 offset:%3" : "=v"(d) : "v"(voff), "s"(sbase), "i"(OFF) : "memory"); }
template <int I> DI void og_issue(OG& o, const char* a0, const char* b0, unsigned as, unsigned bs) {
    gld<(I & 3) * 1024>(o.a[I % OD][0], o.lane16, a0); gld<(I & 3) * 1024>(o.a[I % OD][1], o.lane16, a0 + as);
    gld<(I & 3) * 1024>(o.b[I % OD][0], o.lane16, b0); gld<(I & 3) * 1024>(o.b[I % OD][1], o.lane16, b0 + bs);
}
template <int N, int I> DI void og_wait(OG& o) { asm volatile("s_waitcnt vmcnt(%4)" : "+v"(o.a[I % OD][0]), "+v"(o.a[I % OD][1]), "+v"(o.b[I % OD][0]), "+v"(o.b[I % OD][1]) : "i"(N) : "memory"); }
template <int I> DI void og_mma(OG& o) {
    o.acc[0] = MFMA(o.a[I % OD][0], o.b[I % OD][0], o.acc[0]); o.acc[1] = MFMA(o.a[I % OD][0], o.b[I % OD][1], o.acc[1]);
    o.acc[2] = MFMA(o.a[I % OD][1], o.b[I % OD][0], o.acc[2]); o.acc[3] = MFMA(o.a[I % OD][1], o.b[I % OD][1], o.acc[3]);
    PIN(o.acc[0]); PIN(o.acc[1]); PIN(o.acc[2]); PIN(o.acc[3]);
}
struct OP { const char *ya, *yb, *oa, *ob; };
template <int I> DI void og_issue_step(OG& o, const OP& p) {
    if constexpr (I < 8) og_issue<I>(o, p.ya + (I >> 2) * 4096, p.yb + (I >> 2) * 4096, 65536u, 65536u);
    else if constexpr (I < 40) og_issue<I>(o, p.oa + ((I - 8) >> 2) * 4096, p.ob + ((I - 8) >> 2) * 4096, 262144u, 262144u);
}
DI void og_reduce(OG& o, char* lds, unsigned w, unsigned l, float (&res)[8]) {
    LAS char* L = (LAS char*)lds;
#pragma unroll
    for (int t = 0; t < 4; ++t)
#pragma unroll
        for (int q = 0; q < 4; ++q) *(LAS f32x4*)(L + w * 16384 + (t * 4 + q) * 1024 + l * 16) = (f32x4){o.acc[t][4 * q], o.acc[t][4 * q + 1], o.acc[t][4 * q + 2], o.acc[t][4 * q + 3]};
    bar();
#pragma unroll
    for (int q = 0; q < 2; ++q) {
        f32x4 s = *(const LAS f32x4*)(L + (2 * w + q) * 1024 + l * 16);
#pragma unroll
        for (int v = 1; v < 8; ++v) s += *(const LAS f32x4*)(L + v * 16384 + (2 * w + q) * 1024 + l * 16);
#pragma unroll
        for (int j = 0; j < 4; ++j) res[4 * q + j] = s[j];
    }
}
template <int I> DI void og_step(OG& o, const OP& p) {
    constexpr int rem = 39 - I < OD - 1 ? 39 - I : OD - 1;
    og_wait<4 * rem, I>(o);
    og_mma<I>(o);
    SBAR();
    og_issue_step<I + OD>(o, p);
    SBAR();
}
template <int... Is> DI void og_pro(OG& o, const OP& p, iseq<Is...>) { (og_issue_step<Is>(o, p), ...); }
template <int B, int... Is> DI void og_all(OG& o, const OP& p, iseq<Is...>) { (og_step<B + Is>(o, p), ...); }

__global__ __launch_bounds__(512, 2) void k_out(const Params P) {
    extern __shared__ __attribute__((aligned(16))) char lds[];
    char* ws = P.ws;
    const unsigned tid = threadIdx.x, l = tid & 63, w = __builtin_amdgcn_readfirstlane(tid >> 6);
    const unsigned b = blockIdx.x, xcd = b & 7, idx = b >> 3, rbp = 4 * xcd + (idx >> 3), cb = idx & 7;
    OG o; o.lane16 = l * 16;
    OP p;
    p.ya = ws + WS_HF + ((size_t)(2 * rbp) * 64 + 8 * w) * 1024; p.yb = ws + WS_CF + ((size_t)(2 * cb) * 64 + 8 * w) * 1024;
    p.oa = ws + WS_CTX + ((size_t)(2 * rbp) * 256 + 32 * w) * 1024; p.ob = ws + WS_WOF + ((size_t)(2 * cb) * 256 + 32 * w) * 1024;
#pragma unroll
    for (int t = 0; t < 4; ++t) o.acc[t] = f32x16{};
    const unsigned t = w >> 1, row0 = 64 * rbp + 32 * (t >> 1) + 16 * (w & 1) + 4 * (l >> 5), col = 64 * cb + 32 * (t & 1) + (l & 31);
    unsigned xr[8], dr;
    { const unsigned xo = (row0 * 512 + col) * 2;
      asm volatile("global_load_ushort %0, %1, %2" : "=v"(dr) : "v"(col * 2), "s"(P.dsk) : "memory");
#pragma unroll
      for (int i = 0; i < 8; ++i) asm volatile("global_load_ushort %0, %1, %2 offset:%3" : "=v"(xr[i]) : "v"(xo), "s"((const char*)P.bx + (i >> 2) * 8192), "i"((i & 3) * 1024) : "memory"); }
    og_pro(o, p, make_iseq<OD>{});
    og_all<0>(o, p, make_iseq<8>{});
    asm volatile("" : "+v"(dr), "+v"(xr[0]), "+v"(xr[1]), "+v"(xr[2]), "+v"(xr[3]), "+v"(xr[4]), "+v"(xr[5]), "+v"(xr[6]), "+v"(xr[7]));
    float res[8];
    og_reduce(o, lds, w, l, res);
    unsigned oo = (row0 * 1024 + col) * 4;
#pragma unroll
    for (int i = 0; i < 8; ++i) *(float*)((char*)P.out + (8 * (i >> 2) + (i & 3)) * 4096 + 2048 + oo) = res[i] + __uint_as_float(cvtpk(__uint_as_float(xr[i] << 16) * __uint_as_float(dr << 16), 0.f) << 16);
#pragma unroll
    for (int t2 = 0; t2 < 4; ++t2) o.acc[t2] = f32x16{};
    SBAR();
    og_all<8>(o, p, make_iseq<32>{});
    bar();
    og_reduce(o, lds, w, l, res);
    PIN(oo);
#pragma unroll
    for (int i = 0; i < 8; ++i) *(float*)((char*)P.out + (8 * (i >> 2) + (i & 3)) * 4096 + oo) = res[i];
}

extern "C" void kernel_launch(void* const* d_in, const int* in_sizes, int n_in, void* d_out, int out_size, void* d_ws, size_t ws_size, hipStream_t stream) {
    Params P;
    P.ax = (const u16*)d_in[0]; P.ckv = (const u16*)d_in[1]; P.wq = (const u16*)d_in[2]; P.wuk = (const u16*)d_in[3]; P.wuv = (const u16*)d_in[4]; P.wo = (const u16*)d_in[5];
    P.bx = (const u16*)d_in[6]; P.lre = (const u16*)d_in[7]; P.lim = (const u16*)d_in[8]; P.bre = (const u16*)d_in[9]; P.bim = (const u16*)d_in[10];
    P.cre = (const u16*)d_in[11]; P.cim = (const u16*)d_in[12]; P.logdt = (const u16*)d_in[13]; P.dsk = (const u16*)d_in[14];
    P.out = (float*)d_out; P.ws = (char*)d_ws;
    (void)hipFuncSetAttribute((const void*)k_stage1, hipFuncAttributeMaxDynamicSharedMemorySize, S1_LDS);
    (void)hipFuncSetAttribute((const void*)k_attn, hipFuncAttributeMaxDynamicSharedMemorySize, A_LDS);
    (void)hipFuncSetAttribute((const void*)k_out, hipFuncAttributeMaxDynamicSharedMemorySize, O_LDS);
    k_stage1<<<256, 512, S1_LDS, stream>>>(P);
    k_attn<<<256, 512, A_LDS, stream>>>(P);
    k_out<<<256, 512, O_LDS, stream>>>(P);
}
```

```cpp
#include <hip/hip_runtime.h>
#include <stdint.h>
#include <utility>

typedef unsigned short u16;
typedef short bf16x8 __attribute__((ext_vector_type(8)));
typedef short s16x4 __attribute__((ext_vector_type(4)));
typedef float f32x16 __attribute__((ext_vector_type(16)));
typedef float f32x4 __attribute__((ext_vector_type(4)));
typedef float f32x2 __attribute__((ext_vector_type(2)));
typedef __bf16 bf16x2_t __attribute__((ext_vector_type(2)));
typedef unsigned u32x4 __attribute__((ext_vector_type(4)));
#define LAS __attribute__((address_space(3)))
#define DI __device__ __forceinline__
#define MFMA(a, b, c) __builtin_amdgcn_mfma_f32_32x32x16_bf16((a), (b), (c), 0, 0, 0)
#define SBAR() __builtin_amdgcn_sched_barrier(0)
#define PIN(x) asm volatile("" : "+v"(x))
#define PINS(x) asm volatile("" : "+s"(x))
#define MX3(a, b, c) __builtin_fmaxf(__builtin_fmaxf((a), (b)), (c))
template <int... Is> using iseq = std::integer_sequence<int, Is...>;
template <int N> using make_iseq = std::make_integer_sequence<int, N>;

constexpr size_t WS_KV = 0;
constexpr size_t WS_CTX = 32u << 20;
constexpr size_t WS_WOF = 48u << 20;
constexpr size_t WS_HF = 52u << 20;
constexpr size_t WS_CF = 56u << 20;
constexpr size_t WS_HL = 58u << 20;
constexpr size_t WS_E = 66u << 20;
constexpr size_t WS_CY = 68u << 20;
constexpr size_t WS_FLAG = 67u << 20;
constexpr int LDS_BYTES = 163840;
struct Params {
    const u16 *ax, *ckv, *wq, *wuk, *wuv, *wo, *bx, *lre, *lim, *bre, *bim, *cre, *cim, *logdt, *dsk;
    float* out; char* ws;
};
typedef const Params __attribute__((address_space(4)))* KArg;
struct cplx { float r, i; };
DI cplx cmul(cplx a, cplx b) { return {a.r * b.r - a.i * b.i, a.r * b.i + a.i * b.r}; }
DI cplx cfma(cplx a, cplx b, cplx c) { return {a.r * b.r - a.i * b.i + c.r, a.r * b.i + a.i * b.r + c.i}; }
DI unsigned cvtpk(float lo, float hi) { f32x2 v = {lo, hi}; bf16x2_t b = __builtin_convertvector(v, bf16x2_t); return __builtin_bit_cast(unsigned, b); }
DI u32x4 pack8(const f32x16& x, int s) {
    u32x4 p = {cvtpk(x[8 * s + 0], x[8 * s + 1]), cvtpk(x[8 * s + 2], x[8 * s + 3]), cvtpk(x[8 * s + 4], x[8 * s + 5]), cvtpk(x[8 * s + 6], x[8 * s + 7])};
    return p;
}
template <int OFF> DI void glds16o(unsigned voff, const void* sbase, unsigned ldsb) {
    asm volatile("s_mov_b32 m0, %2\n\ts_nop 0\n\tglobal_load_lds_dwordx4 %0, %1 offset:%3" :: "v"(voff), "s"(sbase), "s"(ldsb), "i"(OFF) : "memory");
}
template <int OFF> DI void glds16n(unsigned voff, const void* sbase) {
    asm volatile("global_load_lds_dwordx4 %0, %1 offset:%2" :: "v"(voff), "s"(sbase), "i"(OFF) : "memory");
}
template <int N> DI void wait_bar() { asm volatile("s_waitcnt vmcnt(%0) lgkmcnt(0)\n\ts_barrier" :: "i"(N) : "memory"); }
DI void bar() { asm volatile("s_waitcnt lgkmcnt(0)\n\ts_barrier" ::: "memory"); }
DI bf16x8 lds_b128(const LAS char* p) { return *(const LAS bf16x8*)p; }
typedef short v4i16_t __attribute__((ext_vector_type(4)));
DI s16x4 lds_tr(const LAS char* p) { return __builtin_bit_cast(s16x4, __builtin_amdgcn_ds_read_tr16_b64_v4i16((LAS v4i16_t*)p)); }
DI bf16x8 cat(s16x4 a, s16x4 b) { return __builtin_shufflevector(a, b, 0, 1, 2, 3, 4, 5, 6, 7); }
template <int OFF> DI void st16s(unsigned voff, const void* sbase, u32x4 v) { asm volatile("global_store_dwordx4 %0, %1, %2 offset:%3 sc1\n\ts_nop 1" :: "v"(voff), "v"(v), "s"(sbase), "i"(OFF) : "memory"); }
template <int OFF> DI void st16kv(unsigned voff, const void* sbase, u32x4 v) { st16s<OFF>(voff, sbase, v); }
DI void st8s(unsigned voff, const void* sbase, f32x2 v) { asm volatile("s_nop 4\n\tglobal_store_dwordx2 %0, %1, %2 sc1" :: "v"(voff), "v"(v), "s"(sbase) : "memory"); }
DI void st4s(unsigned voff, const void* sbase, unsigned v) { asm volatile("s_nop 4\n\tglobal_store_dword %0, %1, %2 sc1" :: "v"(voff), "v"(v), "s"(sbase) : "memory"); }
DI void flag_ld(u32x4& f, unsigned lane16, const char* arr) { asm volatile("s_nop 4\n\tglobal_load_dwordx4 %0, %1, %2 sc1" : "=v"(f) : "v"(lane16), "s"(arr) : "memory"); }
DI bool flag_ok(const u32x4& f, unsigned tag) { return __all(f[0] == tag && f[1] == tag && f[2] == tag && f[3] == tag); }
DI void flag_spin(unsigned lane16, const char* arr, unsigned tag) {
    for (int it = 0; it < 200000; ++it) {
        u32x4 f; asm volatile("s_nop 4\n\tglobal_load_dwordx4 %0, %1, %2 sc1\n\ts_waitcnt vmcnt(0)" : "=v"(f) : "v"(lane16), "s"(arr) : "memory");
        if (flag_ok(f, tag)) break;
        __builtin_amdgcn_s_sleep(2);
    }
}
#define STAMP3(i) do {} while (0)
#define STAMP2(i) do {} while (0)
#define STAMP(i) do {} while (0)

struct GS {
    unsigned ldsb, w, b, lane16, tag, stv;
    const LAS char* L;
    char* ws;
    unsigned voffc[2], voffw;
    const LAS char* crd[4];
    const char* qx; const char* qw;
};

constexpr int U16K = 16384;
constexpr int qpw(int k) { return (k % 3 == 0 ? 5 : k % 3 == 1 ? 2 : 3) * U16K; }
constexpr int qpx(int k) { return (k % 3 == 0 ? 0 : k % 3 == 1 ? 6 : 8) * U16K; }
constexpr int xbu(int q, int part) { return ((q & 1) == 0 ? (part == 0 ? 8 : part == 1 ? 9 : 4) : (part == 0 ? 5 : part == 1 ? 6 : 7)) * U16K; }
constexpr int s1_ev(int st, int i) {
    if (st == 0) return (i == 1 || i == 3) ? 204 + (i >> 1) : (i == 5 || i == 7 || i == 9 || i == 11) ? 108 + ((i - 5) >> 1) : i == 13 ? 206 : i == 15 ? 207 : 0;
    if (st < 6) return (i == 1 || i == 3 || i == 5 || i == 7) ? 100 + 4 * (st + 2) + (i >> 1) : i == 9 ? 200 + 2 * (st + 3) : i == 11 ? 201 + 2 * (st + 3) : (st == 2 && i == 13) ? 700 : 0;
    if (st <= 12 && (i == 1 || i == 2)) return 200 + 2 * (st + 3) + (i - 1);
    if (st == 6) return (i == 5 || i == 7 || i == 9 || i == 11) ? 300 + ((i - 5) >> 1) : 0;
    if (st == 7) return (i >= 4 && i <= 7) ? 304 + (i - 4) : i == 10 ? 500 : 0;
    if (st == 8) return i == 5 ? 501 : (i >= 8 && i <= 14 && (i & 1) == 0) ? 308 + ((i - 8) >> 1) : 0;
    if (st == 9) return i == 12 ? 502 : 0;
    if (st == 10) return i == 5 ? 504 : (i == 3 || i == 4) ? 312 + (i - 3) : (i >= 7 && i <= 13 && (i & 1)) ? 314 + ((i - 7) >> 1) : 0;
    if (st == 11) return (i >= 4 && i <= 14 && (i & 1) == 0) ? 318 + ((i - 4) >> 1) : i == 9 ? 503 : 0;
    if (st == 12) return i == 5 ? 505 : 0;
    if (st == 14) return i == 5 ? 506 : (i == 1 || i == 2) ? 400 + (i - 1) : (i >= 12) ? 402 + (i - 12) : 0;
    if (st == 15) return (i >= 1 && i <= 6) ? 406 + (i - 1) : i >= 8 ? 600 + (i - 8) : 0;
    return 0;
}
constexpr int s1_wait(int st) {
    int tags[256] = {}; int n = 0;
    for (int i = 0; i < 4; ++i) tags[n++] = 100 + i; tags[n++] = 200; tags[n++] = 201;
    for (int i = 0; i < 4; ++i) tags[n++] = 104 + i; tags[n++] = 202; tags[n++] = 203;
    for (int s = 0; s < st; ++s) for (int i = 0; i < 16; ++i) { const int e = s1_ev(s, i); if (e) { tags[n++] = e; if (e >= 500 && e < 600) tags[n++] = e; if (e == 700) { tags[n++] = e; tags[n++] = e; } } }
    const int q = st == 9 ? 0 : st == 10 ? 1 : st == 12 ? 2 : st == 13 ? 3 : -1;
    int last = -1;
    for (int i = 0; i < n; ++i) { const int t = tags[i];
        if (t == 200 + 2 * st || t == 201 + 2 * st || (st < 8 && t >= 100 + 4 * st && t < 104 + 4 * st) || (q >= 0 && t >= 300 + 6 * q && t < 306 + 6 * q) || (st == 5 && t == 700)) last = i; }
    return n - 1 - last;
}
DI void sincos_cw(float x, float& sn, float& cs) {
    const float k = __builtin_rintf(x * 0.6366197723675814f);
    float r = __builtin_fmaf(k, -1.5707963705062866f, x); r = __builtin_fmaf(k, 4.371138828673793e-8f, r); r = __builtin_fmaf(k, 1.7151245100058819e-15f, r);
    const float z = r * r;
    float ps = __builtin_fmaf(z, 2.7557314297e-6f, -1.9841270114e-4f); ps = __builtin_fmaf(ps, z, 8.3333337680e-3f); ps = __builtin_fmaf(ps, z, -1.6666667163e-1f);
    const float s0 = __builtin_fmaf(ps * z, r, r);
    float pc = __builtin_fmaf(z, -2.7557314297e-7f, 2.4801587642e-5f); pc = __builtin_fmaf(pc, z, -1.3888889225e-3f); pc = __builtin_fmaf(pc, z, 4.1666667908e-2f); pc = __builtin_fmaf(pc, z, -0.5f);
    const float c0 = __builtin_fmaf(pc, z, 1.0f);
    const int q = (int)k;
    const float a = (q & 1) ? c0 : s0, b = (q & 1) ? s0 : c0;
    sn = (q & 2) ? -a : a; cs = ((q + 1) & 2) ? -b : b;
}
DI float exp_acc(float x) {
    const float t = x * 1.4426950408889634f, lo = __builtin_fmaf(x, 1.4426950408889634f, -t) + x * 1.9259629911e-8f;
    return __builtin_amdgcn_exp2f(t) * __builtin_fmaf(lo, 0.6931471805599453f, 1.0f);
}
DI cplx ssm_lambar(unsigned lre, unsigned lim, unsigned ldt, cplx& lam) {
    lam = {__uint_as_float(lre << 16), __uint_as_float(lim << 16)};
    const float dt = __uint_as_float(cvtpk(exp_acc(__uint_as_float(ldt << 16)), 0.f) << 16);
    const float e = exp_acc(dt * lam.r); float sn, cs; sincos_cw(dt * lam.i, sn, cs);
    return {e * cs, e * sn};
}
struct S1 {
    unsigned tdummy; const char* tbase;
    unsigned plr, pli, pdt, pg2; const u16 *lre, *lim, *ldt; u32x4 pk[3][2];
    bf16x8 wf[32];
    f32x16 acc[4];
    f32x16 accp[2];
    bf16x8 cf[6];
    f32x16 xacc; bf16x8 xa[4], xb[4];
    cplx lb, l8, hs, cy, hh[8]; float ur[3], ui[3]; f32x2 e2[3];
    const char* cbase;
    const char* wk; const char* wv;
    const char* xsrc; const char* bsrc;
    const LAS char* wrd;
    const char* outp;
    const char* hl; unsigned hloff, sc, uoff, aoff;
};
template <int ST, int P> DI void s1_dma_w1(const GS& G, S1& c) {
    glds16o<0>(G.voffw, ((P < 2) ? c.wk : c.wv) + (size_t)ST * 64 * 8192 + (P & 1) * 128, G.ldsb + 4 * U16K + (ST % 3) * 32768 + G.w * 4096 + P * 1024);
}
template <int U, int E> DI void s1_dma_c1(const GS& G, S1& c) {
    const unsigned dst = G.ldsb + (U & 3) * U16K + G.w * 2048 + E * 1024;
    const unsigned mb = G.w >> 1, r0 = 16 * (G.w & 1) + 8 * E;
    if constexpr (U < 8) glds16o<0>(G.voffc[E], c.cbase + (size_t)(32 * mb + r0) * 1024 + U * 128, dst);
    else { constexpr int v = U - 8; glds16o<0>(G.voffc[E], c.cbase + (size_t)(128 + 32 * (v >> 1) + r0) * 1024 + (v & 1) * 512 + mb * 128, dst); }
}
template <int Q, int P> DI void xb_dma1(const GS& G, S1& c) {
    constexpr int e = P & 1, part = P >> 1;
    if constexpr (part < 2) glds16o<0>(G.voffc[e], c.xsrc + Q * 256 + part * 128 + e * 8192, G.ldsb + xbu(Q, part) + G.w * 2048 + e * 1024);
    else glds16o<0>(G.voffc[e], c.bsrc + Q * 256 + e * 8192, G.ldsb + xbu(Q, 2) + G.w * 2048 + e * 1024);
}
template <int K, int P> DI void qp_dma1(const GS& G) {
    if constexpr (P < 2) glds16o<0>(G.voffw, G.qw + (size_t)K * 64 * 8192 + P * 128, G.ldsb + qpw(K) + G.w * 2048 + P * 1024);
    else glds16o<0>(G.voffc[P & 1], G.qx + K * 128 + (P - 2) * 8192, G.ldsb + qpx(K) + G.w * 4096 + (P - 2) * 1024);
}
template <bool ISV, int T> DI void s1_store(const GS& G, S1& c, const f32x16& a) {
    const char* p = c.outp + (T >> 1) * 32768 + (ISV ? (T & 1) * 2048 : (T & 1) * 8192);
    st16kv<0>(G.lane16, p, pack8(a, 0)); st16kv<1024>(G.lane16, p, pack8(a, 1));
}
template <bool ISV, int T> DI void s1_store_pk(const GS& G, S1& c) {
    const char* p = c.outp + (T >> 1) * 32768 + (ISV ? (T & 1) * 2048 : (T & 1) * 8192);
    st16kv<0>(G.lane16, p, c.pk[T - 1][0]); st16kv<1024>(G.lane16, p, c.pk[T - 1][1]);
}
DI void ld_u16(unsigned& d, unsigned voff, const void* sbase) { asm volatile("s_nop 4\n\tglobal_load_ushort %0, %1, %2" : "=v"(d) : "v"(voff), "s"(sbase) : "memory"); }
DI void touch(unsigned& d, unsigned voff, const void* sbase) { asm volatile("s_nop 4\n\tglobal_load_dword %0, %1, %2" : "+v"(d) : "v"(voff), "s"(sbase) : "memory"); }
constexpr int S1_D = 4, S1_NB = 6;
template <bool ISV, int GI> DI void s1_gap(GS& G, S1& c) {
    constexpr int R = GI + S1_D;
    constexpr int st = R >> 4, i = R & 15;
    if constexpr (R < 256) {
        if constexpr (i == 0) { wait_bar<s1_wait(st)>(); STAMP2(10 + st); }
        constexpr int cs = (st & 3) * U16K;
        constexpr int s = st < 7 ? (i >> 2) : (i & 3), mb = st < 7 ? (i & 3) : (i >> 2);
        if constexpr (st < 8 && mb == 0) { constexpr int wsl = (st % 3) * 32768 + s * 8192; c.wf[4 * st + s] = cat(lds_tr(c.wrd + wsl), lds_tr(c.wrd + wsl + 256)); }
        c.cf[R % S1_NB] = lds_b128(G.crd[s] + cs + mb * 4096);
    }
    if constexpr (GI >= 0) {
        constexpr int sg = GI >> 4, ig = GI & 15;
        if constexpr (sg < 8) {
            constexpr int s = sg < 7 ? ig >> 2 : ig & 3, mb = sg < 7 ? ig & 3 : ig >> 2, ks = 4 * sg + s;
            if constexpr (ks == 0) c.acc[mb] = f32x16{};
            c.acc[mb] = ISV ? MFMA(c.cf[GI % S1_NB], c.wf[ks], c.acc[mb]) : MFMA(c.wf[ks], c.cf[GI % S1_NB], c.acc[mb]);
            PIN(c.acc[mb]);
        } else {
            constexpr int u = sg - 8, ai = (u >> 1) & 1, ks = 16 * (u & 1) + ig;
            if constexpr (ks == 0) c.accp[ai] = f32x16{};
            c.accp[ai] = ISV ? MFMA(c.cf[GI % S1_NB], c.wf[ks], c.accp[ai]) : MFMA(c.wf[ks], c.cf[GI % S1_NB], c.accp[ai]);
            PIN(c.accp[ai]);
        }
    }
    if constexpr (R < 256) {
        if constexpr (st >= 1 && st <= 4 && i == 14) { if ((G.w >> 1) == st - 1) touch(c.tdummy, G.lane16 * 8, c.tbase); }
        if constexpr (st == 8 && i == 0) PIN(c.tdummy);
        if constexpr (st == 5 && i == 13) {
            asm volatile("" : "+v"(c.plr), "+v"(c.pli), "+v"(c.pdt));
            cplx lam; c.lb = ssm_lambar(c.plr, c.pli, c.pdt, lam); PIN(c.lb.r); PIN(c.lb.i);
        }
        if constexpr ((st == 7 && i == 14) || (st == 8 && (i == 3 || i == 7))) { constexpr int t = st == 7 ? 1 : i == 3 ? 2 : 3; c.pk[t - 1][0] = pack8(c.acc[t], 0); c.pk[t - 1][1] = pack8(c.acc[t], 1); PIN(c.pk[t - 1][0]); PIN(c.pk[t - 1][1]); }
        constexpr int q = st == 9 ? 0 : st == 10 ? 1 : st == 12 ? 2 : st == 13 ? 3 : -1;
        if constexpr (q >= 0) {
            constexpr int o = (st & 1) ? 1 : 6, jr = i - o, jm = i - o - 2;
            if constexpr (i == 0) { PINS(c.aoff); PINS(c.uoff); }
            if constexpr (jr >= 0 && jr < 8) { c.xa[jr & 3] = lds_b128(G.crd[jr & 3] + c.aoff + xbu(q, jr >> 2)); c.xb[jr & 3] = lds_b128(G.crd[jr & 3] + c.uoff + xbu(q, 2) + (jr >> 2) * 8192); }
            if constexpr (jm >= 0 && jm < 8) { if constexpr (q == 0 && jm == 0) c.xacc = f32x16{}; c.xacc = MFMA(c.xa[jm & 3], c.xb[jm & 3], c.xacc); PIN(c.xacc); }
            if constexpr (st == 13 && i >= 13) {
                constexpr int r0 = i == 13 ? 0 : i == 14 ? 5 : 10, r1 = i == 13 ? 5 : i == 14 ? 10 : 16;
                LAS float* Uw = (LAS float*)((LAS char*)G.L + 8 * U16K + G.w * 4096 + (G.lane16 >> 9) * 512 + ((G.lane16 >> 2) & 124));
#pragma unroll
                for (int r = r0; r < r1; ++r) Uw[((r & 3) + 8 * (r >> 2)) * 32] = c.xacc[r];
            }
        }
        if constexpr (st == 14) {
            const LAS float* Ur = (const LAS float*)(G.L + 8 * U16K + c.sc * 1024 + ((G.lane16 >> 2) & 124));
            LAS char* E2 = (LAS char*)G.L + 4 * U16K + c.sc * 256 + ((G.lane16 >> 1) & 248);
            if constexpr (i >= 1 && i <= 8) { c.ur[i % 3] = Ur[(i - 1) * 32]; c.ui[i % 3] = Ur[4096 + (i - 1) * 32]; }
            if constexpr (i == 0) *(LAS f32x2*)E2 = (f32x2){0.f, 0.f};
            if constexpr (i >= 3 && i <= 10) { constexpr int k = i - 3; const cplx bu = {c.ur[(i - 2) % 3], c.ui[(i - 2) % 3]};
                if constexpr (k == 0) c.hs = bu; else c.hs = cfma(c.lb, c.hs, bu);
                PIN(c.hs.r); PIN(c.hs.i); c.hh[k] = c.hs; }
            if constexpr (i == 12) *(LAS f32x2*)(E2 + 4096) = (f32x2){c.hs.r, c.hs.i};
            if constexpr (i >= 11 && i <= 13) { if constexpr (i == 11) c.l8 = c.lb; c.l8 = cmul(c.l8, c.l8); PIN(c.l8.r); PIN(c.l8.i); }
        }
        if constexpr (st == 15) {
            const LAS f32x2* E2 = (const LAS f32x2*)(G.L + 4 * U16K + c.sc * 256 + ((G.lane16 >> 1) & 248));
            if constexpr (i < 15) c.e2[i % 3] = E2[(i + 1) * 32];
            if constexpr (i >= 2) { const cplx e = {c.e2[(i - 2) % 3][0], c.e2[(i - 2) % 3][1]}; if constexpr (i == 2) c.cy = e; else c.cy = cfma(c.l8, c.cy, e); PIN(c.cy.r); PIN(c.cy.i); }
        }
        constexpr int ev = s1_ev(st, i);
        if constexpr (ev >= 100 && ev < 200) s1_dma_w1<(ev - 100) / 4, (ev - 100) % 4>(G, c);
        if constexpr (ev >= 200 && ev < 300) s1_dma_c1<(ev - 200) / 2, (ev - 200) % 2>(G, c);
        if constexpr (ev >= 300 && ev < 400) xb_dma1<(ev - 300) / 6, (ev - 300) % 6>(G, c);
        if constexpr (ev >= 400 && ev < 500) qp_dma1<(ev - 400) / 6, (ev - 400) % 6>(G);
        if constexpr (ev == 500) s1_store<ISV, 0>(G, c, c.acc[0]);
        if constexpr (ev > 500 && ev < 504) s1_store_pk<ISV, ev - 500>(G, c);
        if constexpr (ev == 700) { ld_u16(c.plr, c.pg2, c.lre); ld_u16(c.pli, c.pg2, c.lim); ld_u16(c.pdt, c.pg2, c.ldt); }
        if constexpr (ev >= 504 && ev < 600) s1_store<ISV, ev - 500>(G, c, c.accp[(ev - 504) & 1]);
        if constexpr (ev >= 600 && ev < 700) { constexpr int k = ev - 600;
            asm volatile("global_store_dwordx2 %0, %1, %2" :: "v"(c.hloff), "v"((f32x2){c.hh[k].r, c.hh[k].i}), "s"(c.hl + k * 4096) : "memory"); }
    }
    if constexpr (R == 256) { c.cy = cfma(c.l8, c.cy, cplx{c.e2[14 % 3][0], c.e2[14 % 3][1]}); PIN(c.cy.r); PIN(c.cy.i); }
    if constexpr (GI == 255) s1_store<ISV, 7>(G, c, c.accp[1]);
    SBAR();
}
template <bool ISV, int... Gs> DI void s1_all(GS& G, S1& c, iseq<Gs...>) { (s1_gap<ISV, Gs - S1_D>(G, c), ...); }

constexpr float QSCALE = 0.08838834764831845f * 1.4426950408889634f;
constexpr int A_Z = 49152, A_ZB = 98304, A_ML = 81920;
DI unsigned kaddr(int t) { return 98304u + (unsigned)((t + 2) & 3) * 16384u; }
DI unsigned vaddr(int t) { return (unsigned)(t - 3 * ((t * 11) >> 5)) * 16384u; }
template <bool FIRST, int OFF> DI void glds16c(unsigned voff, const void* sbase, unsigned ldsb, unsigned long long em) {
    if constexpr (FIRST) asm volatile("s_mov_b32 m0, %2\n\ts_mov_b64 exec, %3\n\tglobal_load_lds_dwordx4 %0, %1 sc1\n\ts_mov_b64 exec, -1" :: "v"(voff), "s"(sbase), "s"(ldsb), "s"(em) : "memory");
    else asm volatile("s_mov_b64 exec, %2\n\tglobal_load_lds_dwordx4 %0, %1 offset:%3 sc1\n\ts_mov_b64 exec, -1" :: "v"(voff), "s"(sbase), "s"(em), "i"(OFF) : "memory");
}

struct AT {
    bf16x8 qf[8];
    f32x16 o[4];
    f32x16 s[2];
    f32x16 negm;
    u32x4 pf[4];
    bf16x8 fb[8];
    float m, l, rm, a[4];
    bf16x8 ones;
    bool lo16;
    const char* ks; const char* vs; unsigned kd, vd, special, t0, t1; unsigned long long km, vm; int p;
    unsigned tab0, tab1;
    unsigned ldsb, ldsr, w, g, rg, lane16;
    const char* kvsrc;
    const LAS char* L;
    const LAS char* kb;
    const LAS char* vb0; const LAS char* vb1;
    const char* ctx;
    int n, rhi, rb1, rb2;
};
constexpr int AD = 5;
template <int F> DI void at_rd(AT& c) {
    if constexpr (F < 16) c.fb[F & 7] = lds_b128(c.kb + ((F & 1) * 8 + (F >> 1)) * 1024);
    else if constexpr (F < 32) { constexpr int ks = (F - 16) >> 2, dt = (F - 16) & 3; c.fb[F & 7] = lds_b128((dt < 2 ? c.vb0 : c.vb1) + ((dt & 1) * 4 + ks) * 1024); }
}
DI void at_tables(AT& c, int lane) {
    const int p = lane - 1, n = c.n, g = (int)c.g, y = 2 * p + g + 2;
    const int tr_k = p + 1 < n ? p + 1 : 2 * (p + 1) - n + g, tr_v = p < n ? p : 2 * p - n + g;
    const int tk = y >= 2 * n - 3 ? y + 3 - n : (g ? (y + 3) >> 1 : 99), tv = y >= 2 * n ? y - n : (g ? 99 : y >> 1);
    c.tab0 = kaddr(tr_k) | (tk < 34 - n ? 1u : 0u) | ((unsigned)(tk & 31) << 20) | (kaddr(tk) >> 10);
    c.tab1 = (vaddr(tr_v & 31) + g * 8192u) | (tv < 34 - n ? 1u : 0u) | ((unsigned)(tv & 31) << 20) | (vaddr(tv & 31) >> 10);
}
template <int J> DI void at_setup(AT& c) {
    if constexpr (J == 0) { c.p += 1; PINS(c.p); c.t0 = __builtin_amdgcn_readlane(c.tab0, c.p + 1); c.t1 = __builtin_amdgcn_readlane(c.tab1, c.p + 1); PINS(c.t0); PINS(c.t1); }
    if constexpr (J == 2) { c.kb = c.L + c.lane16 + (c.t0 & 0x3c000u); PIN(c.kb); }
    if constexpr (J == 3) { c.kd = c.ldsr + ((c.t0 & 0xf0u) << 10); PINS(c.kd); }
    if constexpr (J == 4) { c.vd = c.ldsr + ((c.t1 & 0xf0u) << 10); PINS(c.vd); }
    if constexpr (J == 5) { c.km = (c.t0 & 1u) ? ~0ull : 0ull; PINS(c.km); }
    if constexpr (J == 6) { c.vm = (c.t1 & 1u) ? ~0ull : 0ull; PINS(c.vm); }
    if constexpr (J == 7) { c.ks = c.kvsrc + ((c.t0 >> 20) << 15); PINS(c.ks); }
    if constexpr (J == 8) { c.vs = c.kvsrc + (((c.t1 >> 20) << 15) + 16384u); PINS(c.vs); }
    if constexpr (J == 12) { c.vb0 = c.L + c.lane16 + (c.t1 & 0x3e000u); PIN(c.vb0); }
    if constexpr (J == 13) { c.vb1 = c.L + c.lane16 + ((c.t1 & 0x3e000u) ^ 8192u); PIN(c.vb1); }
}
template <int... Js> DI void at_setup_all(AT& c, iseq<Js...>) { (at_setup<Js>(c), ...); }
template <bool RDV, int F> DI void mm_gap(AT& c) {
    if constexpr (F + AD < 16 || RDV) at_rd<F + AD>(c);
    if constexpr (F < 16) {
        constexpr int kb2 = F & 1, d0 = F >> 1;
        if constexpr (d0 == 0) asm volatile("v_mfma_f32_32x32x16_bf16 %0, %1, %2, %3" : "=&v"(c.s[kb2]) : "v"(c.fb[F & 7]), "v"(c.qf[0]), "v"(c.negm));
        else c.s[kb2] = MFMA(c.fb[F & 7], c.qf[d0], c.s[kb2]);
        PIN(c.s[kb2]);
        if constexpr ((F & 1) == 0) { if constexpr (F < 8) glds16c<F == 0, (F >> 1) * 1024>(c.lane16, c.vs, c.vd, c.vm); else glds16c<F == 8, ((F - 8) >> 1) * 1024>(c.lane16, c.ks, c.kd, c.km); }
    } else {
        constexpr int ks = (F - 16) >> 2, dt = (F - 16) & 3;
        c.o[dt] = MFMA(c.fb[F & 7], __builtin_bit_cast(bf16x8, c.pf[ks]), c.o[dt]); PIN(c.o[dt]);
        at_setup<F - 16>(c);
        if constexpr (F == 28) bar();
    }
    SBAR();
}
template <bool RDV, int B, int... Fs> DI void mm_gaps(AT& c, iseq<Fs...>) { (mm_gap<RDV, B + Fs>(c), ...); }
template <int B, int... Fs> DI void pre_rd(AT& c, iseq<Fs...>) { (at_rd<B + Fs>(c), ...); }
DI void at_fix(AT& c, bool first) {
    float a0 = MX3(c.s[0][0], c.s[0][1], c.s[0][2]), a1 = MX3(c.s[0][8], c.s[0][9], c.s[0][10]), a2 = MX3(c.s[1][0], c.s[1][1], c.s[1][2]), a3 = MX3(c.s[1][8], c.s[1][9], c.s[1][10]);
    a0 = MX3(a0, c.s[0][3], c.s[0][4]); a1 = MX3(a1, c.s[0][11], c.s[0][12]); a2 = MX3(a2, c.s[1][3], c.s[1][4]); a3 = MX3(a3, c.s[1][11], c.s[1][12]);
    a0 = MX3(a0, c.s[0][5], c.s[0][6]); a1 = MX3(a1, c.s[0][13], c.s[0][14]); a2 = MX3(a2, c.s[1][5], c.s[1][6]); a3 = MX3(a3, c.s[1][13], c.s[1][14]);
    a0 = MX3(a0, c.s[0][7], c.s[0][15]); a2 = MX3(a2, c.s[1][7], c.s[1][15]); a0 = MX3(a0, a1, a2); a0 = __builtin_fmaxf(a0, a3);
    auto rr = __builtin_amdgcn_permlane32_swap(__float_as_uint(a0), __float_as_uint(a0), false, false);
    const float rm = __builtin_fmaxf(__uint_as_float(rr[0]), __uint_as_float(rr[1]));
    const float dl = __builtin_fmaxf(rm, first ? -1e30f : 0.f);
    c.m += dl;
#pragma unroll
    for (int r = 0; r < 16; ++r) { c.s[0][r] -= dl; c.s[1][r] -= dl; c.negm[r] = -c.m; }
    if (!first) {
        const float f = __builtin_amdgcn_exp2f(-dl); c.l *= f;
#pragma unroll
        for (int dt = 0; dt < 4; ++dt) c.o[dt] *= f;
    }
}
DI void at_mask(AT& c, int kbase) {
#pragma unroll
    for (int kb2 = 0; kb2 < 2; ++kb2) {
        const int d = kbase + kb2 - (int)c.rg;
        if (d >= 0) {
            const unsigned long long full = d > 0 ? ~0ull : 0ull;
#pragma unroll
            for (int r = 0; r < 16; ++r) {
                const int cc = (r & 3) + 8 * (r >> 2);
                unsigned long long mk = (((1ull << cc) - 1) | (((1ull << (cc + 4)) - 1) << 32)) | full;
                asm volatile("v_cndmask_b32_e64 %0, %0, %1, %2" : "+v"(c.s[kb2][r]) : "v"(-INFINITY), "s"(mk));
            }
        }
    }
}
DI void at_vphase(AT& c) {
    const int p = c.p;
    pre_rd<0>(c, make_iseq<AD>{});
    SBAR();
    bool fix = false, fin = false;
    if (__builtin_expect((c.special >> p) & 1, 0)) {
        fin = c.g == 0 && p == c.n;
        if (fin) {
            asm volatile("s_waitcnt vmcnt(0)" ::: "memory");
            const float rl = __builtin_amdgcn_rcpf(c.rm);
            const char* op = c.ctx + (size_t)c.rb1 * 262144;
#pragma unroll
            for (int dt = 0; dt < 4; ++dt) { const f32x16 v = c.o[dt] * rl; st16s<0>(c.lane16, op + dt * 2048, pack8(v, 0)); st16s<1024>(c.lane16, op + dt * 2048, pack8(v, 1)); c.o[dt] = f32x16{}; }
            SBAR();
        }
        at_mask(c, p == 16 ? 2 * (int)c.g : (c.g == 0 && p < c.n && p >= c.n - 2) ? 2 * (p - c.n + 2) : -8);
        fix = p == 0 || fin;
    }
    const bool first = fix;
    float own;
    for (;;) {
        if (__builtin_expect(fix, 0)) at_fix(c, first);
#pragma unroll
        for (int k = 0; k < 4; ++k) {
#pragma unroll
            for (int j = 0; j < 4; ++j)
                c.pf[k][j] = cvtpk(__builtin_amdgcn_exp2f(c.s[k >> 1][8 * (k & 1) + 2 * j]), __builtin_amdgcn_exp2f(c.s[k >> 1][8 * (k & 1) + 2 * j + 1]));
            PIN(c.pf[k]);
        }
        SBAR();
        __builtin_amdgcn_s_setprio(3);
        f32x4 lt = {0.f, 0.f, 0.f, 0.f};
#pragma unroll
        for (int k = 0; k < 4; ++k) lt = __builtin_amdgcn_mfma_f32_16x16x32_bf16(c.ones, __builtin_bit_cast(bf16x8, c.pf[k]), lt, 0, 0, 0);
        __builtin_amdgcn_s_setprio(0);
        own = c.lo16 ? lt[0] : lt[1];
        if (__builtin_expect(fix || !__any(!(own <= 1.0995116e12f)), 1)) break;
        fix = true;
    }
    c.l += own;
    if (__builtin_expect(c.g == 0 && p == c.n - 1, 0)) {
#pragma unroll
        for (int i = 0; i < 8; ++i) c.qf[i] = lds_b128(c.L + A_Z + c.rg * 8192 + c.lane16 + i * 1024);
        c.m = 0.f; c.negm = f32x16{}; c.rm = c.l; c.l = 0.f;
    }
    PIN(c.negm);
    if (!fin) asm volatile("s_waitcnt vmcnt(0)" ::: "memory");
    bar();
}

struct QP {
    f32x16 acc[4];
    bf16x8 xf[2], wf[4];
    u32x4 fl, cv; unsigned cfo; const char* wo; const void* cs;
    const LAS char* wrd;
    const char* ksrc;
};
DI bool flag_ok_head(const u32x4& f, const GS& G) {
    const unsigned e = G.b & 3, v = e == 0 ? f[0] : e == 1 ? f[1] : e == 2 ? f[2] : f[3];
    return __all(((G.lane16 >> 4) & 7) != ((G.b & 31) >> 2) || v == G.tag);
}
DI void flag_spin_head(const GS& G) {
    for (int it = 0; it < 200000; ++it) {
        u32x4 f; asm volatile("s_nop 4\n\tglobal_load_dwordx4 %0, %1, %2 sc1\n\ts_waitcnt vmcnt(0)" : "=v"(f) : "v"(G.lane16), "s"(G.ws + WS_FLAG + 1024) : "memory");
        if (flag_ok_head(f, G)) break;
        __builtin_amdgcn_s_sleep(2);
    }
}
constexpr int QD = 3;
template <int GI> DI void qp_gap(GS& G, QP& q) {
    constexpr int R = GI + QD;
    if constexpr (R < 128) {
        constexpr int st = R >> 4, i = R & 15, s = i >> 2, dt = i & 3;
        if constexpr (i == 0) {
            STAMP3(10 + 2 * st); wait_bar<(st < 2 ? 17 : st < 7 ? 6 : 4)>(); STAMP3(11 + 2 * st);
            if constexpr (st == 2) { if (G.w == 0 && G.lane16 == 0) st4s(G.b * 4, G.ws + WS_FLAG + 1024, G.tag); }
            if constexpr (st == 4) { if (G.w == 0) { flag_ld(q.fl, G.lane16, G.ws + WS_FLAG + 1024); if (G.lane16 == 0) st4s(G.b * 4, G.ws + WS_FLAG + 4096, G.tag); } }
            if constexpr (st == 5) { if (G.w == 0) { PIN(q.fl); if (!flag_ok_head(q.fl, G)) flag_spin_head(G); } }
        }
        if constexpr (dt == 0) q.xf[s & 1] = lds_b128(G.crd[s] + G.w * 4096 + qpx(st));
        q.wf[R & 3] = cat(lds_tr(q.wrd + qpw(st) + s * 4096 + dt * 512), lds_tr(q.wrd + qpw(st) + s * 4096 + dt * 512 + 256));
    }
    if constexpr (GI >= 0) {
        constexpr int s = (GI & 15) >> 2, dt = GI & 3;
        if constexpr (GI < 4) q.acc[dt] = f32x16{};
        q.acc[dt] = MFMA(q.wf[GI & 3], q.xf[s & 1], q.acc[dt]); PIN(q.acc[dt]);
    }
    if constexpr (R < 128) {
        constexpr int st = R >> 4, i = R & 15;
        if constexpr (st + 2 < 8 && (i & 1) == 1 && i < 12) qp_dma1<st + 2, (i >> 1)>(G);
        if constexpr (st == 0 && i == 0) {
            const unsigned l = G.lane16 >> 4, vo = ((l >> 2) & 7) * 1024 + (l >> 5) * 64 + (l & 3) * 16;
            glds16o<0>(vo, q.wo, G.ldsb + 4 * U16K + G.w * 2048); glds16o<0>(vo, q.wo + 8192, G.ldsb + 4 * U16K + G.w * 2048 + 1024);
            const unsigned cn = 8 * (G.b >> 2) + (l & 7), chk = 16 * (G.b & 3) + 8 * (G.w & 1) + (l >> 3);
            q.cfo = ((cn >> 5) * 64 + ((G.w >> 1) & 1) * 32 + (chk >> 1)) * 1024 + ((cn & 31) + 32 * (chk & 1)) * 16;
            asm volatile("s_nop 4\n\tglobal_load_dwordx4 %0, %1, %2" : "=v"(q.cv) : "v"(cn * 1024 + chk * 16), "s"(q.cs) : "memory");
        }
        if constexpr (st == 2 && i == 0) { PIN(q.cv); if (G.w < 4) st16s<0>(q.cfo, G.ws + WS_CF, q.cv); }
        if constexpr (st == 2 && (i == 2 || i == 4)) {
            constexpr int nb = (i - 2) >> 1; const unsigned l = G.lane16 >> 4;
            const LAS char* tp = G.L + 4 * U16K + G.w * 2048 + (4 * (l >> 5) + ((l >> 2) & 3)) * 64 + ((l >> 4) & 1) * 32 + (l & 3) * 8;
            st16s<0>(G.lane16, G.ws + WS_WOF + ((size_t)(2 * G.w + nb) * 256 + G.b) * 1024, __builtin_bit_cast(u32x4, cat(lds_tr(tp + nb * 512), lds_tr(tp + nb * 512 + 1024))));
        }
        if constexpr (st == 6 && (i == 1 || i == 3 || i == 5 || i == 7)) {
            glds16c<true, 0>(G.lane16, q.ksrc + (i >> 2) * 32768 + ((i >> 1) & 1) * 1024, G.ldsb + 98304u + (2 + (i >> 2)) * 16384u + G.w * 2048 + ((i >> 1) & 1) * 1024, ~0ull);
        }
    }
    SBAR();
}
template <int... Gs> DI void qp_all(GS& G, QP& q, iseq<Gs...>) { (qp_gap<Gs - QD>(G, q), ...); }

constexpr int OD = 8;
struct OG { bf16x8 a[OD][2], b[OD][2]; f32x16 acc[4]; u32x4 fl; unsigned lane16; const char *ya, *yb, *oa, *ob; };
template <int OFF> DI void gld(bf16x8& d, unsigned voff, const void* sbase) { asm volatile("global_load_dwordx4 %0, %1, %2 offset:%3" : "=v"(d) : "v"(voff), "s"(sbase), "i"(OFF) : "memory"); }
template <int OFF> DI void glds(bf16x8& d, unsigned voff, const void* sbase) { asm volatile("global_load_dwordx4 %0, %1, %2 offset:%3 sc1" : "=v"(d) : "v"(voff), "s"(sbase), "i"(OFF) : "memory"); }
template <int I> DI void og_issue_a(OG& o) {
    if constexpr (I < 32) { const char* p = o.oa + (I >> 2) * 4096; glds<(I & 3) * 1024>(o.a[I % OD][0], o.lane16, p); glds<(I & 3) * 1024>(o.a[I % OD][1], o.lane16, p + 262144); }
    else if constexpr (I < 40) { const char* p = o.ya + ((I - 32) >> 2) * 4096; glds<(I & 3) * 1024>(o.a[I % OD][0], o.lane16, p); glds<(I & 3) * 1024>(o.a[I % OD][1], o.lane16, p + 65536); }
}
template <int I> DI void og_issue_b(OG& o) {
    if constexpr (I < 32) { const char* p = o.ob + (I >> 2) * 4096; glds<(I & 3) * 1024>(o.b[I % OD][0], o.lane16, p); glds<(I & 3) * 1024>(o.b[I % OD][1], o.lane16, p + 262144); }
    else if constexpr (I < 40) { const char* p = o.yb + ((I - 32) >> 2) * 4096; glds<(I & 3) * 1024>(o.b[I % OD][0], o.lane16, p); glds<(I & 3) * 1024>(o.b[I % OD][1], o.lane16, p + 65536); }
}
template <int N, int I> DI void og_wait(OG& o) { asm volatile("s_waitcnt vmcnt(%4)" : "+v"(o.a[I % OD][0]), "+v"(o.a[I % OD][1]), "+v"(o.b[I % OD][0]), "+v"(o.b[I % OD][1]) : "i"(N) : "memory"); }
DI void og_reduce(OG& o, const GS& G, float (&res)[8]) {
    LAS char* L = (LAS char*)G.L + G.lane16;
#pragma unroll
    for (int t = 0; t < 4; ++t)
#pragma unroll
        for (int q = 0; q < 4; ++q) *(LAS f32x4*)(L + G.w * 16384 + (t * 4 + q) * 1024) = (f32x4){o.acc[t][4 * q], o.acc[t][4 * q + 1], o.acc[t][4 * q + 2], o.acc[t][4 * q + 3]};
    bar();
#pragma unroll
    for (int q = 0; q < 2; ++q) {
        f32x4 s = *(const LAS f32x4*)(L + (2 * G.w + q) * 1024);
#pragma unroll
        for (int v = 1; v < 8; ++v) s += *(const LAS f32x4*)(L + v * 16384 + (2 * G.w + q) * 1024);
#pragma unroll
        for (int j = 0; j < 4; ++j) res[4 * q + j] = s[j];
        SBAR();
    }
}
template <int I> DI void og_step(OG& o, const GS& G) {
    constexpr int rem = 39 - I < OD - 1 ? 39 - I : OD - 1;
    og_wait<(I < OD ? 2 * (OD - 1 - I) + 4 * I : 4 * rem), I>(o);
    o.acc[0] = MFMA(o.a[I % OD][0], o.b[I % OD][0], o.acc[0]); o.acc[1] = MFMA(o.a[I % OD][0], o.b[I % OD][1], o.acc[1]);
    o.acc[2] = MFMA(o.a[I % OD][1], o.b[I % OD][0], o.acc[2]); o.acc[3] = MFMA(o.a[I % OD][1], o.b[I % OD][1], o.acc[3]);
    PIN(o.acc[0]); PIN(o.acc[1]); PIN(o.acc[2]); PIN(o.acc[3]);
    SBAR();
    if constexpr (I == 12) flag_ld(o.fl, o.lane16, G.ws + WS_FLAG + 2048);
    if constexpr (I == 23) { PIN(o.fl); if (!flag_ok(o.fl, G.tag)) flag_spin(o.lane16, G.ws + WS_FLAG + 2048, G.tag); }
    og_issue_a<I + OD>(o); og_issue_b<I + OD>(o);
    SBAR();
}
template <int... Is> DI void og_pro_a(OG& o, iseq<Is...>) { (og_issue_a<Is>(o), ...); }
template <int... Is> DI void og_pro_b(OG& o, iseq<Is...>) { (og_issue_b<Is>(o), ...); }
template <int B, int... Is> DI void og_all(OG& o, const GS& G, iseq<Is...>) { (og_step<B + Is>(o, G), ...); }

__global__ __launch_bounds__(512, 2) void k_fused(const Params P_) {
    extern __shared__ __attribute__((aligned(16))) char lds[];
    KArg KP = (KArg)__builtin_amdgcn_kernarg_segment_ptr();
#define P (*KP)
    char* ws = P.ws;
    const unsigned tid = threadIdx.x, l = tid & 63, w = __builtin_amdgcn_readfirstlane(tid >> 6);
    const unsigned b = blockIdx.x, xcd = b & 7, idx = b >> 3, h = 4 * xcd + (idx & 3), kb = idx >> 2, pi = idx >> 2;
    const unsigned ch = 2 * xcd + (idx >> 4), gq = idx & 15;
    const unsigned g = w >> 2, rg = w & 3;
    const int rb1 = 4 * (int)pi + (int)rg, rb2 = 4 * (15 - (int)pi) + (int)rg;
    GS G;
    G.w = w; G.b = b; G.ldsb = (unsigned)(uintptr_t)(LAS char*)lds; G.L = (const LAS char*)lds; G.lane16 = l * 16; G.ws = ws; G.stv = 0;
    STAMP(0);
    {
        const unsigned r = l & 31, hi = l >> 5;
#pragma unroll
        for (int e = 0; e < 2; ++e) G.voffc[e] = (l >> 3) * 1024 + (((l & 7) ^ ((4 * e + (l >> 4)) & 7)) * 16);
        G.voffw = ((l >> 2) & 7) * 8192 + (l >> 5) * 64 + (l & 3) * 16;
#pragma unroll
        for (int s = 0; s < 4; ++s) G.crd[s] = G.L + r * 128 + (((2 * s + hi) ^ ((r >> 1) & 7)) * 16);
        G.qx = (const char*)P.ax + (size_t)(g ? rb2 : rb1) * 32768; G.qw = (const char*)P.wq + h * 256 + (size_t)w * 8 * 8192;
    }
    {
        const unsigned hi = l >> 5;
        S1 c;
        c.cbase = (const char*)P.ckv + (size_t)kb * 256 * 1024;
        c.wk = (const char*)P.wuk + h * 256 + (size_t)w * 8 * 8192; c.wv = (const char*)P.wuv + h * 256 + (size_t)w * 8 * 8192;
        c.wrd = G.L + 4 * U16K + w * 512 + hi * 4096 + ((l >> 2) & 3) * 64 + ((l >> 4) & 1) * 32 + (l & 3) * 8;
        c.outp = ws + WS_KV + ((size_t)h * 32 + 4 * kb) * 32768 + (w < 4 ? w * 2048 : 16384 + (w - 4) * 4096);
        c.xsrc = (const char*)P.bx + (size_t)(128 * ch + 16 * w) * 1024;
        c.bsrc = (const char*)(((w >> 1) & 1) ? P.bim : P.bre) + (size_t)(32 * gq + 16 * (w & 1)) * 1024 + (w >> 2) * 128;
        c.aoff = (w & 3) * 4096; c.uoff = (w >> 2) * 4096; c.sc = tid >> 5;
        const unsigned pg = 32 * gq + (l & 31);
        c.hl = ws + WS_HL + (size_t)(128 * ch) * 4096; c.hloff = c.sc * 32768 + pg * 8;
        c.pg2 = pg * 2; c.lre = P.lre; c.lim = P.lim; c.ldt = P.logdt;
        s1_dma_w1<0, 0>(G, c); s1_dma_w1<0, 1>(G, c); s1_dma_w1<0, 2>(G, c); s1_dma_w1<0, 3>(G, c); s1_dma_c1<0, 0>(G, c); s1_dma_c1<0, 1>(G, c);
        s1_dma_w1<1, 0>(G, c); s1_dma_w1<1, 1>(G, c); s1_dma_w1<1, 2>(G, c); s1_dma_w1<1, 3>(G, c); s1_dma_c1<1, 0>(G, c); s1_dma_c1<1, 1>(G, c);
        c.tdummy = 0;
        c.tbase = w == 0 ? (const char*)P.bx + (size_t)b * 8192 : w == 1 ? (const char*)((b & 64) ? P.bim : P.bre) + (size_t)(b & 63) * 8192 : w == 2 ? (const char*)P.ckv + (size_t)b * 8192
                : w == 3 ? (const char*)P.ax + (size_t)b * 8192 : w < 6 ? (const char*)P.wq + (size_t)b * 16384 + (w & 1) * 8192 : (const char*)P.wo + (size_t)b * 16384 + (w & 1) * 8192;
        { unsigned nonce; asm volatile("s_load_dword %0, %1, 0x0\n\ts_waitcnt lgkmcnt(0)" : "=s"(nonce) : "s"(ws + WS_FLAG) : "memory"); G.tag = nonce + 1u; }
        SBAR();
        STAMP(1);
        if (w < 4) s1_all<false>(G, c, make_iseq<256 + S1_D>{}); else s1_all<true>(G, c, make_iseq<256 + S1_D>{});
        asm volatile("s_nop 4\n\tglobal_store_dwordx2 %0, %1, %2" :: "v"(tid * 8), "v"((f32x2){c.cy.r, c.cy.i}), "s"(ws + WS_CY + b * 4096) : "memory");
        if (c.sc == 15) { const cplx e = cfma(c.l8, c.cy, c.hs); st8s(c.pg2 * 4, ws + WS_E + ch * 4096, (f32x2){e.r, e.i}); }
        SBAR();
    }
    STAMP(2);
    AT c;
    {
        const unsigned hi = l >> 5;
        QP q;
        q.wrd = G.L + hi * 2048 + ((l >> 2) & 3) * 64 + ((l >> 4) & 1) * 32 + (l & 3) * 8;
        PINS(KP);
        q.wo = (const char*)P.wo + (size_t)b * 16384 + w * 128; q.cs = ((w >> 1) & 1) ? P.cim : P.cre;
        q.ksrc = ws + WS_KV + (size_t)h * 32 * 32768 + w * 2048;
        c.w = w; c.g = g; c.rg = rg; c.ldsb = G.ldsb; c.L = G.L; c.lane16 = G.lane16; c.ldsr = G.ldsb + rg * 4096;
        c.kvsrc = ws + WS_KV + (size_t)h * 32 * 32768 + rg * 4096;
        c.ctx = ws + WS_CTX + (size_t)h * 8192;
        c.rb1 = rb1; c.rb2 = rb2; c.n = 2 * (int)pi + 2;
#pragma unroll
        for (int dt = 0; dt < 4; ++dt) { c.o[dt] = f32x16{}; PIN(c.o[dt]); }
        c.negm = f32x16{}; PIN(c.negm); c.m = 0.f; c.l = 0.f; c.rm = 1.f;
        { const short one = (((l >> 4) ^ l) & 1) ? (short)0 : (short)0x3F80; c.ones = (bf16x8){one, one, one, one, one, one, one, one}; PIN(c.ones); c.lo16 = (l & 16) == 0; }
        c.special = 1u | (1u << 16) | (g ? 0u : (7u << (2 * pi)));
        at_tables(c, (int)l & 31);
        c.p = -2; at_setup_all(c, make_iseq<16>{});
        SBAR();
        qp_all(G, q, make_iseq<128 + QD>{});
#pragma unroll
        for (int dt = 0; dt < 4; ++dt) {
            f32x16 v = q.acc[dt] * QSCALE;
            c.qf[2 * dt] = __builtin_bit_cast(bf16x8, pack8(v, 0)); c.qf[2 * dt + 1] = __builtin_bit_cast(bf16x8, pack8(v, 1));
        }
    }
    STAMP(3);
    wait_bar<0>();
    if (g) {
        LAS char* qp_ = (LAS char*)c.L + A_Z + rg * 8192 + c.lane16;
#pragma unroll
        for (int i = 0; i < 8; ++i) *(LAS bf16x8*)(qp_ + i * 1024) = c.qf[i];
        pre_rd<0>(c, make_iseq<AD>{});
        bar();
    } else pre_rd<0>(c, make_iseq<AD>{});
    {
        __builtin_amdgcn_s_setprio(1);
        mm_gaps<false, 0>(c, make_iseq<16>{});
        __builtin_amdgcn_s_setprio(0);
        at_setup_all(c, make_iseq<16>{});
        bar();
    }
    for (int p = 0; p <= 16; ++p) {
        at_vphase(c);
        __builtin_amdgcn_s_setprio(1);
        mm_gaps<true, 0>(c, make_iseq<32>{});
        __builtin_amdgcn_s_setprio(0);
    }
    STAMP(4);
    PINS(KP);
    unsigned tz = threadIdx.x; PIN(tz);
    const unsigned sc = tz >> 5, pl = tz & 31, pg = 32 * gq + pl;
    f32x2 hv[8], ev[15], cyv; unsigned plr, pli, pdt;
    flag_spin(G.lane16, ws + WS_FLAG + 4096, G.tag);
    {
        const char* hl = ws + WS_HL + (size_t)(128 * ch) * 4096; const unsigned hloff = sc * 32768 + pg * 8;
#pragma unroll
        for (int i = 0; i < 8; ++i) asm volatile("global_load_dwordx2 %0, %1, %2" : "=v"(hv[i]) : "v"(hloff), "s"(hl + i * 4096) : "memory");
#pragma unroll
        for (int i = 0; i < 15; ++i) asm volatile("global_load_dwordx2 %0, %1, %2 sc1" : "=v"(ev[i]) : "v"(pg * 8), "s"(ws + WS_E + i * 4096) : "memory");
        asm volatile("s_nop 4\n\tglobal_load_dwordx2 %0, %1, %2" : "=v"(cyv) : "v"(tz * 8), "s"(ws + WS_CY + b * 4096) : "memory");
        ld_u16(plr, pg * 2, P.lre); ld_u16(pli, pg * 2, P.lim); ld_u16(pdt, pg * 2, P.logdt);
    }
    {
        LAS char* z = (LAS char*)c.L + (g ? A_ZB : A_Z) + rg * 8192 + c.lane16;
#pragma unroll
        for (int i = 0; i < 4; ++i) { *(LAS f32x4*)(z + i * 1024) = (f32x4){c.o[2][4 * i], c.o[2][4 * i + 1], c.o[2][4 * i + 2], c.o[2][4 * i + 3]};
                                      *(LAS f32x4*)(z + 4096 + i * 1024) = (f32x4){c.o[3][4 * i], c.o[3][4 * i + 1], c.o[3][4 * i + 2], c.o[3][4 * i + 3]}; }
        *(LAS f32x2*)((LAS char*)c.L + A_ML + g * 2048 + rg * 512 + (c.lane16 >> 1)) = (f32x2){c.m, c.l};
        if (g == 0) bar();
        bar();
        const f32x2 ml = *(const LAS f32x2*)(c.L + A_ML + (1 - g) * 2048 + rg * 512 + (c.lane16 >> 1));
        const float mm = __builtin_fmaxf(c.m, ml[0]);
        const float f0 = __builtin_amdgcn_exp2f(c.m - mm), f1 = __builtin_amdgcn_exp2f(ml[0] - mm);
        const float rl = __builtin_amdgcn_rcpf(c.l * f0 + ml[1] * f1);
        const float w0 = f0 * rl, w1 = f1 * rl;
        const LAS char* zr = c.L + (g ? A_Z : A_ZB) + rg * 8192 + c.lane16;
        const char* op = c.ctx + (size_t)rb2 * 262144 + g * 4096;
#pragma unroll
        for (int t2 = 0; t2 < 2; ++t2) {
            f32x16 ra;
#pragma unroll
            for (int i = 0; i < 4; ++i) { const f32x4 pa = *(const LAS f32x4*)(zr + t2 * 4096 + i * 1024);
#pragma unroll
                for (int j = 0; j < 4; ++j) ra[4 * i + j] = c.o[t2][4 * i + j] * w0 + pa[j] * w1; }
            st16s<0>(c.lane16, op + t2 * 2048, pack8(ra, 0)); st16s<1024>(c.lane16, op + t2 * 2048, pack8(ra, 1));
        }
    }
    STAMP(5);
    {
        asm volatile("s_waitcnt vmcnt(4)" : "+v"(hv[0]), "+v"(hv[1]), "+v"(hv[2]), "+v"(hv[3]), "+v"(hv[4]), "+v"(hv[5]), "+v"(hv[6]), "+v"(hv[7]), "+v"(plr), "+v"(pli), "+v"(pdt), "+v"(cyv) :: "memory");
        asm volatile("" : "+v"(ev[0]), "+v"(ev[1]), "+v"(ev[2]), "+v"(ev[3]), "+v"(ev[4]), "+v"(ev[5]), "+v"(ev[6]), "+v"(ev[7]), "+v"(ev[8]), "+v"(ev[9]), "+v"(ev[10]), "+v"(ev[11]), "+v"(ev[12]), "+v"(ev[13]), "+v"(ev[14]));
        cplx lam; const cplx lb = ssm_lambar(plr, pli, pdt, lam);
        cplx pw = cmul(lb, lb); pw = cmul(pw, pw); pw = cmul(pw, pw);
        cplx ps = {1.f, 0.f};
#pragma unroll
        for (int bit = 0; bit < 4; ++bit) { const cplx t = cmul(ps, pw); if ((sc >> bit) & 1) ps = t; pw = cmul(pw, pw); }
        cplx hin = {0.f, 0.f};
#pragma unroll
        for (int i = 0; i < 15; ++i) if (i < (int)ch) hin = cfma(pw, hin, cplx{ev[i][0], ev[i][1]});
        cplx gg = cfma(ps, hin, cplx{cyv[0], cyv[1]});
        const float den = 2.f * __builtin_amdgcn_rcpf(lam.r * lam.r + lam.i * lam.i);
        const cplx coef = {((lb.r - 1.f) * lam.r + lb.i * lam.i) * den, (lb.i * lam.r - (lb.r - 1.f) * lam.i) * den};
        LAS u16* T = (LAS u16*)lds;
#pragma unroll
        for (int i = 0; i < 8; ++i) { gg = cmul(lb, gg); const cplx hf = cmul(coef, cplx{hv[i][0] + gg.r, hv[i][1] + gg.i}); const unsigned pk = cvtpk(hf.r, -hf.i);
            T[(8 * sc + i) * 32 + pl] = (u16)pk; T[4096 + (8 * sc + i) * 32 + pl] = (u16)(pk >> 16); }
        wait_bar<0>();
        if (tz == 0) st4s(b * 4, ws + WS_FLAG + 3072, G.tag);
#pragma unroll
        for (int j = 0; j < 2; ++j) {
            const unsigned t = tz & 127, qq = tz >> 7;
            const u32x4 v = *(const LAS u32x4*)((const LAS char*)T + j * 8192 + t * 64 + qq * 16);
            st16s<0>(((t >> 5) * 64 + (qq >> 1)) * 1024 + ((t & 31) + 32 * (qq & 1)) * 16, ws + WS_HF + ((size_t)(4 * ch) * 64 + j * 32 + 2 * gq) * 1024, v);
        }
        SBAR();
    }
    STAMP(6);
    {
        PINS(KP);
        const unsigned rbp = 4 * xcd + (idx >> 3), cb = idx & 7, lz = tz & 63;
        OG o; o.lane16 = G.lane16;
        o.ya = ws + WS_HF + ((size_t)(2 * rbp) * 64 + 8 * w) * 1024; o.yb = ws + WS_CF + ((size_t)(2 * cb) * 64 + 8 * w) * 1024;
        o.oa = ws + WS_CTX + ((size_t)(2 * rbp) * 256 + 32 * w) * 1024; o.ob = ws + WS_WOF + ((size_t)(2 * cb) * 256 + 32 * w) * 1024;
#pragma unroll
        for (int t = 0; t < 4; ++t) o.acc[t] = f32x16{};
        const unsigned t = w >> 1, row0 = 64 * rbp + 32 * (t >> 1) + 16 * (w & 1) + 4 * (lz >> 5), col = 64 * cb + 32 * (t & 1) + (lz & 31);
        og_pro_b(o, make_iseq<OD>{});
        if (w == 0) flag_spin(G.lane16, ws + WS_FLAG + 3072, G.tag);
        wait_bar<0>();
        if (tz == 0) { st4s(b * 4, ws + WS_FLAG + 2048, G.tag); if (b == 0) st4s(0u, ws + WS_FLAG, G.tag); }
        STAMP(7);
        og_pro_a(o, make_iseq<OD>{});
        og_all<0>(o, G, make_iseq<32>{});
        STAMP(8);
        float res[8];
        og_reduce(o, G, res);
        PINS(KP);
        unsigned oo = (row0 * 1024 + col) * 4;
#pragma unroll
        for (int i = 0; i < 8; ++i) *(float*)((char*)P.out + (8 * (i >> 2) + (i & 3)) * 4096 + oo) = res[i];
#pragma unroll
        for (int t2 = 0; t2 < 4; ++t2) o.acc[t2] = f32x16{};
        unsigned xr[8], dr;
        { const unsigned xo = (row0 * 512 + col) * 2;
          ld_u16(dr, col * 2, P.dsk);
#pragma unroll
          for (int i = 0; i < 8; ++i) asm volatile("s_nop 4\n\tglobal_load_ushort %0, %1, %2 offset:%3" : "=v"(xr[i]) : "v"(xo), "s"((const char*)P.bx + (i >> 2) * 8192), "i"((i & 3) * 1024) : "memory"); }
        SBAR();
        og_all<32>(o, G, make_iseq<8>{});
        asm volatile("" : "+v"(dr), "+v"(xr[0]), "+v"(xr[1]), "+v"(xr[2]), "+v"(xr[3]), "+v"(xr[4]), "+v"(xr[5]), "+v"(xr[6]), "+v"(xr[7]));
        bar();
        og_reduce(o, G, res);
        PIN(oo);
#pragma unroll
        for (int i = 0; i < 8; ++i) *(float*)((char*)P.out + (8 * (i >> 2) + (i & 3)) * 4096 + 2048 + oo) = res[i] + __uint_as_float(cvtpk(__uint_as_float(xr[i] << 16) * __uint_as_float(dr << 16), 0.f) << 16);
    }
    STAMP(9);
#undef P
}

extern "C" void kernel_launch(void* const* d_in, const int* in_sizes, int n_in, void* d_out, int out_size, void* d_ws, size_t ws_size, hipStream_t stream) {
    Params P;
    P.ax = (const u16*)d_in[0]; P.ckv = (const u16*)d_in[1]; P.wq = (const u16*)d_in[2]; P.wuk = (const u16*)d_in[3]; P.wuv = (const u16*)d_in[4]; P.wo = (const u16*)d_in[5];
    P.bx = (const u16*)d_in[6]; P.lre = (const u16*)d_in[7]; P.lim = (const u16*)d_in[8]; P.bre = (const u16*)d_in[9]; P.bim = (const u16*)d_in[10];
    P.cre = (const u16*)d_in[11]; P.cim = (const u16*)d_in[12]; P.logdt = (const u16*)d_in[13]; P.dsk = (const u16*)d_in[14];
    P.out = (float*)d_out; P.ws = (char*)d_ws;
    (void)hipFuncSetAttribute((const void*)k_fused, hipFuncAttributeMaxDynamicSharedMemorySize, LDS_BYTES);
    k_fused<<<256, 512, LDS_BYTES, stream>>>(P);
}
```
